```python
import torch

def _softmax(x, axis=-1):
    return torch.softmax(x, dim=axis)
def _gelu(x):
    return torch.nn.functional.gelu(x, approximate="tanh")
def _silu(x):
    return torch.nn.functional.silu(x)
def _sigmoid(x):
    return torch.sigmoid(x)
def _layernorm(x, axis=-1, eps=1e-5):
    m = x.mean(dim=axis, keepdim=True)
    v = x.var(dim=axis, keepdim=True, unbiased=False)
    return (x - m) / torch.sqrt(v + eps)
def _scatter_add(tgt, idx, src, axis=0):
    return tgt.clone().index_add_(axis, idx, src)
def _segment_sum(src, seg, n):
    out = torch.zeros((n,) + src.shape[1:], dtype=src.dtype)
    return out.index_add_(0, seg, src)
def _pad(x, pw):
    flat = tuple(v for pair in reversed(pw) for v in pair)
    return torch.nn.functional.pad(x, flat)

kernel_name = 'gated_mix__gqa_block_cached__dense_activation_silu__decode'

def setup_inputs():

    _w = (lambda a: (lambda t: t.to(torch.bfloat16) if t.is_floating_point() else t)(torch.as_tensor(a)))
    def _A_setup():
        B, Sq, D = 2, 1, 448
        Hq, Hkv, Sk, Dh = 16, 8, 32768, 256
        assert Hq % Hkv == 0, (Hq, Hkv)
        return {
            "x": _w((lambda *s: torch.randn(*s, dtype=torch.bfloat16) * 0.05)(B, Sq, D)),
            "k_cache": _w((lambda *s: torch.randn(*s, dtype=torch.bfloat16) * 6.4)(B, Hkv, Sk, Dh)),
            "v_cache": _w((lambda *s: torch.randn(*s, dtype=torch.bfloat16) * 0.8)(B, Hkv, Sk, Dh)),
            "wq": _w((lambda *s: torch.randn(*s, dtype=torch.bfloat16) * 6.4)(D, Hq * Dh) * (D ** -0.5)),
            "wo": _w((lambda *s: torch.randn(*s, dtype=torch.bfloat16) * 0.8)(Hq * Dh, D) * ((Hq * Dh) ** -0.5)),
        }
    _A_in = _A_setup()

    def _B_setup():
        B, S, D, F = 2, 1, 448, 1792
        return {
            "x": _w((lambda *s: torch.randn(*s, dtype=torch.bfloat16) * 0.05)(B, S, D)),
            "w1": _w((lambda *s: torch.randn(*s, dtype=torch.bfloat16) * 12.8)(D, F) * (D ** -0.5)),
            "w2": _w((lambda *s: torch.randn(*s, dtype=torch.bfloat16) * 0.2)(F, D) * (F ** -0.5)),
        }
    _B_in = _B_setup()

    return {
        "A_x": _A_in["x"],
        "A_k_cache": _A_in["k_cache"],
        "A_v_cache": _A_in["v_cache"],
        "A_wq": _A_in["wq"],
        "A_wo": _A_in["wo"],
        "B_x": _B_in["x"],
        "B_w1": _B_in["w1"],
        "B_w2": _B_in["w2"],
        "gate_w": _w((lambda *s: torch.randn(*s, dtype=torch.bfloat16) * 0.2)(448) * 16.0),
    }

def reference(A_x, A_k_cache, A_v_cache, A_wq, A_wo, B_x, B_w1, B_w2, gate_w):
    def _A_ref(x, k_cache, v_cache, wq, wo):
        B, Sq, D = x.shape
        Hq, Dh = 16, 256
        Hkv = k_cache.shape[1]
        rep = Hq // Hkv
        q = (lambda x, *d: x.permute(*d))((x @ wq).reshape(B, Sq, Hq, Dh), 0, 2, 1, 3)
        k = (lambda x, n, axis: torch.repeat_interleave(x, n, dim=axis))(k_cache, rep, axis=1)
        v = (lambda x, n, axis: torch.repeat_interleave(x, n, dim=axis))(v_cache, rep, axis=1)
        scores = torch.einsum("bhqd,bhkd->bhqk", q, k) * (Dh ** -0.5)
        p = _softmax(scores, axis=-1)
        o = torch.einsum("bhqk,bhkd->bhqd", p, v)
        return (lambda x, *d: x.permute(*d))(o, 0, 2, 1, 3).reshape(B, Sq, Hq * Dh) @ wo

    def _B_ref(x, w1, w2):
        h = torch.einsum("bsd,df->bsf", x, w1)
        h = _silu(h)
        out = torch.einsum("bsf,fd->bsd", h, w2)
        return out

    x = A_x
    g = _sigmoid((x * gate_w).sum(-1, keepdims=True))
    a = _A_ref(x, A_k_cache, A_v_cache, A_wq, A_wo)
    b = _B_ref(x, B_w1, B_w2)
    return g * a + (1 - g) * b
```

```cpp
#include <hip/hip_runtime.h>
#include <hip/hip_bf16.h>
#include <stdint.h>

#define GAS __attribute__((address_space(1)))
#define LAS __attribute__((address_space(3)))
typedef unsigned short u16;
typedef unsigned long long u64;
typedef unsigned u32x4 __attribute__((ext_vector_type(4)));
typedef unsigned u32x2 __attribute__((ext_vector_type(2)));
typedef float f32x4 __attribute__((ext_vector_type(4)));
typedef short s16x4 __attribute__((ext_vector_type(4)));
typedef __bf16 bf16x8 __attribute__((ext_vector_type(8)));
typedef __bf16 bf16x2 __attribute__((ext_vector_type(2)));

namespace {

constexpr int DM = 448, HQ = 16, HKV = 8, SK = 32768, DH = 256, FF = 1792;
constexpr int NWG = 256, NSPLIT = 16, TK = 16, NT = SK / NSPLIT / TK;
constexpr int NCONS = 4, NLOAD = 2, NTHREADS = 7 * 64;
constexpr int PSTR = 1056;
constexpr int TILEB = 16 * PSTR;
constexpr int NSLOT = 8;
constexpr int L_RING = 0;
constexpr int L_MRG = NSLOT * TILEB;
constexpr int L_ML = L_MRG + 4 * 2 * 256 * 4;
constexpr int L_YP = L_ML + 64;
constexpr int L_BP = L_YP + 4 * DM * 4;
constexpr int L_Q = L_BP + DM * 4;
constexpr int L_QP = L_Q + 1024;
constexpr int L_CTL = L_QP + 512;
constexpr int LDS_BYTES = L_CTL + 256;
static_assert(LDS_BYTES <= 160 * 1024, "LDS");
enum { C_FULL = 0, C_FREE = 8, C_QCNT = 16, C_QRDY = 17, C_G = 18 };
constexpr size_t W_EPOCH = 0;
constexpr size_t W_QG = 4096;
constexpr size_t W_INBOX = W_QG + 16 * 512 * 8;
constexpr int INBOXB = 272 * 16;
constexpr size_t W_END = W_INBOX + (size_t)NWG * INBOXB;
constexpr unsigned SENT = 0xFFFFFFFFu;
constexpr long long TCAP = 5000000;

__device__ __forceinline__ u64 rt() { return __builtin_amdgcn_s_memrealtime(); }
__device__ __forceinline__ float bf2f(unsigned v) { return __uint_as_float(v << 16); }
__device__ __forceinline__ float bflo(unsigned v) { return __uint_as_float(v << 16); }
__device__ __forceinline__ float bfhi(unsigned v) { return __uint_as_float(v & 0xffff0000u); }
__device__ __forceinline__ unsigned f2bf(float f) { return (unsigned)__builtin_bit_cast(u16, (__bf16)f); }
__device__ __forceinline__ float rbf(float f) { return (float)(__bf16)f; }
__device__ __forceinline__ unsigned pack2(float lo, float hi) { bf16x2 r = {(__bf16)lo, (__bf16)hi}; return __builtin_bit_cast(unsigned, r); }
__device__ __forceinline__ float dot2(unsigned a, unsigned b, float c) {
  return __builtin_amdgcn_fdot2_f32_bf16(__builtin_bit_cast(bf16x2, a), __builtin_bit_cast(bf16x2, b), c, false);
}
template <int CTRL> __device__ __forceinline__ float dpp(float x) {
  return __builtin_bit_cast(float, __builtin_amdgcn_mov_dpp(__builtin_bit_cast(int, x), CTRL, 0xf, 0xf, true));
}
constexpr int ROR2 = 0x122, ROR4 = 0x124, ROR8 = 0x128, QX1 = 0xB1, QB0 = 0x00, QB1 = 0x55, QB2 = 0xAA;
__device__ __forceinline__ float xrow_max(float x) {
  auto s = __builtin_amdgcn_permlane16_swap(__float_as_uint(x), __float_as_uint(x), false, false);
  x = fmaxf(__uint_as_float(s[0]), __uint_as_float(s[1]));
  auto t = __builtin_amdgcn_permlane32_swap(__float_as_uint(x), __float_as_uint(x), false, false);
  return fmaxf(__uint_as_float(t[0]), __uint_as_float(t[1]));
}
__device__ __forceinline__ float xrow_sum(float x) {
  auto s = __builtin_amdgcn_permlane16_swap(__float_as_uint(x), __float_as_uint(x), false, false);
  x = __uint_as_float(s[0]) + __uint_as_float(s[1]);
  auto t = __builtin_amdgcn_permlane32_swap(__float_as_uint(x), __float_as_uint(x), false, false);
  return __uint_as_float(t[0]) + __uint_as_float(t[1]);
}
__device__ __forceinline__ unsigned nosent(float f) { unsigned u = __float_as_uint(f); return u == SENT ? 0x7fc00000u : u; }
__device__ __forceinline__ void lds_wait() { asm volatile("s_waitcnt lgkmcnt(0)" ::: "memory"); }
__device__ __forceinline__ void wg_barrier() { asm volatile("s_waitcnt lgkmcnt(0)\n\ts_barrier" ::: "memory"); }
__device__ __forceinline__ u32x4 ldnt(const void* p) { return __builtin_nontemporal_load((const GAS u32x4*)p); }

__device__ __forceinline__ bool wait_lds_ge(volatile LAS unsigned* w, unsigned need) {
  if ((unsigned)__builtin_amdgcn_readfirstlane(*w) >= need) return true;
  const u64 t0 = rt();
  for (;;) {
    __builtin_amdgcn_s_sleep(1);
    if ((unsigned)__builtin_amdgcn_readfirstlane(*w) >= need) return true;
    if ((long long)(rt() - t0) > TCAP) return false;
  }
}

__device__ __forceinline__ void issue_tile(const char* kb, const char* vb, unsigned dst, const unsigned (&voff)[8]) {
  unsigned keep;
  asm volatile(
      "s_mov_b32 %0, m0\n\t"
      "s_mov_b32 m0, %1\n\ts_nop 0\n\tglobal_load_lds_dwordx4 %4, %2 nt\n\t"
      "s_add_u32 m0, m0, 1056\n\ts_nop 0\n\tglobal_load_lds_dwordx4 %5, %2 nt\n\t"
      "s_add_u32 m0, m0, 1056\n\ts_nop 0\n\tglobal_load_lds_dwordx4 %6, %2 nt\n\t"
      "s_add_u32 m0, m0, 1056\n\ts_nop 0\n\tglobal_load_lds_dwordx4 %7, %2 nt\n\t"
      "s_add_u32 m0, m0, 1056\n\ts_nop 0\n\tglobal_load_lds_dwordx4 %8, %2 nt\n\t"
      "s_add_u32 m0, m0, 1056\n\ts_nop 0\n\tglobal_load_lds_dwordx4 %9, %2 nt\n\t"
      "s_add_u32 m0, m0, 1056\n\ts_nop 0\n\tglobal_load_lds_dwordx4 %10, %2 nt\n\t"
      "s_add_u32 m0, m0, 1056\n\ts_nop 0\n\tglobal_load_lds_dwordx4 %11, %2 nt\n\t"
      "s_add_u32 m0, m0, 1056\n\ts_nop 0\n\tglobal_load_lds_dwordx4 %4, %3 nt\n\t"
      "s_add_u32 m0, m0, 1056\n\ts_nop 0\n\tglobal_load_lds_dwordx4 %5, %3 nt\n\t"
      "s_add_u32 m0, m0, 1056\n\ts_nop 0\n\tglobal_load_lds_dwordx4 %6, %3 nt\n\t"
      "s_add_u32 m0, m0, 1056\n\ts_nop 0\n\tglobal_load_lds_dwordx4 %7, %3 nt\n\t"
      "s_add_u32 m0, m0, 1056\n\ts_nop 0\n\tglobal_load_lds_dwordx4 %8, %3 nt\n\t"
      "s_add_u32 m0, m0, 1056\n\ts_nop 0\n\tglobal_load_lds_dwordx4 %9, %3 nt\n\t"
      "s_add_u32 m0, m0, 1056\n\ts_nop 0\n\tglobal_load_lds_dwordx4 %10, %3 nt\n\t"
      "s_add_u32 m0, m0, 1056\n\ts_nop 0\n\tglobal_load_lds_dwordx4 %11, %3 nt\n\t"
      "s_mov_b32 m0, %0"
      : "=&s"(keep)
      : "s"(dst), "s"(kb), "s"(vb), "v"(voff[0]), "v"(voff[1]), "v"(voff[2]), "v"(voff[3]), "v"(voff[4]), "v"(voff[5]), "v"(voff[6]), "v"(voff[7])
      : "memory", "scc");
}
__device__ __forceinline__ void issue_wo(const char* src, unsigned dst, unsigned voff) {
#pragma unroll
  for (int i = 0; i < 14; ++i) {
    unsigned keep;
    asm volatile("s_mov_b32 %0, m0\n\ts_mov_b32 m0, %1\n\ts_nop 0\n\tglobal_load_lds_dwordx4 %3, %2 nt\n\ts_mov_b32 m0, %0"
                 : "=&s"(keep) : "s"(dst + i * 1024), "s"(src + i * 1024), "v"(voff) : "memory");
  }
}

__global__ void __launch_bounds__(NTHREADS, 2)
fused_decode(const u16* __restrict__ x, const char* __restrict__ Kc, const char* __restrict__ Vc, const u16* __restrict__ wq,
             const char* __restrict__ wo, const u16* __restrict__ w1, const u16* __restrict__ w2, const u16* __restrict__ gw,
             u16* __restrict__ out, char* __restrict__ ws) {
  __shared__ __attribute__((aligned(16))) unsigned char lds_raw[LDS_BYTES];
  LAS unsigned char* L = (LAS unsigned char*)lds_raw;
  volatile LAS unsigned* ctl = (volatile LAS unsigned*)(L + L_CTL);
  const int tid = threadIdx.x, lane = tid & 63, wave = __builtin_amdgcn_readfirstlane(tid >> 6);
  const int wg = blockIdx.x, grp = wg >> 4, j = wg & 15, b = grp >> 3, kvh = grp & 7;
  const u16* xb = x + b * DM;
  GAS u64* epoch = (GAS u64*)(ws + W_EPOCH + b * 256);
  GAS u64* qg = (GAS u64*)(ws + W_QG);
  const __amdgpu_buffer_rsrc_t rs_in = __builtin_amdgcn_make_buffer_rsrc(ws + W_INBOX, 0, NWG * INBOXB, 0x00020000);

  if (wave < NCONS) {
    const u64 ep = __hip_atomic_load(epoch, __ATOMIC_RELAXED, __HIP_MEMORY_SCOPE_AGENT);
    {
      const u32x4 s4 = {SENT, SENT, SENT, SENT};
      __builtin_amdgcn_raw_buffer_store_b128(s4, rs_in, wg * INBOXB + tid * 16, 0, 16);
      if (tid < 16) __builtin_amdgcn_raw_buffer_store_b128(s4, rs_in, wg * INBOXB + (256 + tid) * 16, 0, 16);
    }
    const int cb = 2 * (j & 7) + (j >> 3), rg = lane >> 2, cc = lane & 3;
    u32x4 wv[7]; unsigned xv[7];
#pragma unroll
    for (int i = 0; i < 7; ++i) {
      const int row = 112 * wave + 16 * i + rg;
      wv[i] = ldnt(wq + (size_t)row * (HQ * DH) + kvh * 512 + cb * 32 + cc * 8);
      xv[i] = xb[row];
    }
    if (tid < 32) ctl[tid] = 0;
    wg_barrier();
    {
      float a[8];
#pragma unroll
      for (int e = 0; e < 8; ++e) a[e] = 0.f;
#pragma unroll
      for (int i = 0; i < 7; ++i) {
        const float xf = bf2f(xv[i]);
#pragma unroll
        for (int e = 0; e < 4; ++e) { a[2 * e] = fmaf(xf, bflo(wv[i][e]), a[2 * e]); a[2 * e + 1] = fmaf(xf, bfhi(wv[i][e]), a[2 * e + 1]); }
      }
#pragma unroll
      for (int e = 0; e < 8; ++e) { float t = a[e]; t += dpp<ROR4>(t); t += dpp<ROR8>(t); a[e] = xrow_sum(t); }
      LAS float* qp = (LAS float*)(L + L_QP) + wave * 32;
      if (lane < 4) { *(LAS f32x4*)(qp + lane * 8) = f32x4{a[0], a[1], a[2], a[3]}; *(LAS f32x4*)(qp + lane * 8 + 4) = f32x4{a[4], a[5], a[6], a[7]}; }
      asm volatile("s_waitcnt vmcnt(0) lgkmcnt(0)" ::: "memory");
      unsigned old = 0;
      if (lane == 0) old = __hip_atomic_fetch_add((LAS unsigned*)(L + L_CTL) + C_QCNT, 1u, __ATOMIC_RELAXED, __HIP_MEMORY_SCOPE_WORKGROUP);
      old = __builtin_amdgcn_readfirstlane(old);
      const u64 tag = ((ep + 1) & 0xFFFFFFFFFFFFull) << 16;
      if (old == NCONS - 1) {
        asm volatile("" ::: "memory");
        if (lane < 32) {
          const LAS float* q0 = (const LAS float*)(L + L_QP);
          const float s = (q0[lane] + q0[32 + lane]) + (q0[64 + lane] + q0[96 + lane]);
          __hip_atomic_store(qg + grp * 512 + cb * 32 + lane, tag | f2bf(s), __ATOMIC_RELAXED, __HIP_MEMORY_SCOPE_AGENT);
        }
      }
      if (wave == 0) {
        const u64 t0 = rt();
        for (;;) {
          bool ok = true; u64 v[8];
#pragma unroll
          for (int i = 0; i < 8; ++i) { v[i] = __hip_atomic_load(qg + grp * 512 + i * 64 + lane, __ATOMIC_RELAXED, __HIP_MEMORY_SCOPE_AGENT); ok &= (v[i] >> 16) == (tag >> 16); }
          if (__all(ok)) {
#pragma unroll
            for (int i = 0; i < 8; ++i) *(LAS u16*)(L + L_Q + (i * 64 + lane) * 2) = (u16)v[i];
            break;
          }
          if ((long long)(rt() - t0) > TCAP) return;
          __builtin_amdgcn_s_sleep(1);
        }
        lds_wait();
        ctl[C_QRDY] = 1;
      }
    }
    if (!wait_lds_ge(ctl + C_QRDY, 1)) return;
    asm volatile("" ::: "memory");
    const int g = lane >> 4, c = lane & 15;
    bf16x8 qf[8];
#pragma unroll
    for (int s = 0; s < 8; ++s) qf[s] = *(const LAS bf16x8*)(L + L_Q + ((c & 1) * 256 + 32 * s + 8 * g) * 2);

    const int odd = c & 1;
    const unsigned kA = (c >> 1) * PSTR + odd * 512 + g * 16 + odd * 128, kB = (c >> 1) * PSTR + odd * 512 + g * 16 + (odd ^ 1) * 128;
    const int vkey = 4 * g + (c >> 2), vodd = vkey & 1;
    const unsigned vbase = 8 * PSTR + (vkey >> 1) * PSTR + vodd * 512 + (c & 3) * 8;
    const unsigned vA = vbase + vodd * 128, vB = vbase + (vodd ^ 1) * 128;
    constexpr float C2 = 0.0625f * 1.4426950408889634f;

    f32x4 acc[16];
#pragma unroll
    for (int t = 0; t < 16; ++t) acc[t] = f32x4{0.f, 0.f, 0.f, 0.f};
    float m = -INFINITY, l = 0.f;

    for (int n = wave; n < NT; n += NCONS) {
      const int slot = n & (NSLOT - 1);
      if (!wait_lds_ge(ctl + C_FULL + slot, n + 1)) return;
      asm volatile("" ::: "memory");
      LAS unsigned char* S = L + L_RING + slot * TILEB;
      bf16x8 kf[8]; s16x4 vf[16];
#pragma unroll
      for (int s = 0; s < 8; ++s) kf[s] = *(const LAS bf16x8*)(S + ((s & 2) ? kB : kA) + 64 * (s & 1) + 256 * (s >> 2));
#pragma unroll
      for (int t = 0; t < 16; ++t) vf[t] = __builtin_amdgcn_ds_read_tr16_b64_v4i16((LAS s16x4*)(S + ((t & 4) ? vB : vA) + 32 * (t & 3) + 256 * (t >> 3)));
      f32x4 sc = {0.f, 0.f, 0.f, 0.f};
#pragma unroll
      for (int s = 0; s < 8; ++s) sc = __builtin_amdgcn_mfma_f32_16x16x32_bf16(kf[s], qf[s], sc, 0, 0, 0);
      lds_wait();
      ctl[C_FREE + slot] = n + 1;
      const float tmax = fmaxf(fmaxf(sc[0], sc[1]), fmaxf(sc[2], sc[3])) * C2;
      if (__any(tmax > m)) {
        const float mn = fmaxf(m, xrow_max(tmax));
        const float al = __builtin_amdgcn_exp2f(m - mn);
        l *= al;
#pragma unroll
        for (int t = 0; t < 16; ++t) acc[t] *= al;
        m = mn;
      }
      const float p0 = __builtin_amdgcn_exp2f(fmaf(sc[0], C2, -m)), p1 = __builtin_amdgcn_exp2f(fmaf(sc[1], C2, -m));
      const float p2 = __builtin_amdgcn_exp2f(fmaf(sc[2], C2, -m)), p3 = __builtin_amdgcn_exp2f(fmaf(sc[3], C2, -m));
      l += (p0 + p1) + (p2 + p3);
      const u32x2 pp = {pack2(p0, p1), pack2(p2, p3)};
      const s16x4 pf = __builtin_bit_cast(s16x4, pp);
#pragma unroll
      for (int t = 0; t < 16; ++t) acc[t] = __builtin_amdgcn_mfma_f32_16x16x16bf16_1k(vf[t], pf, acc[t], 0, 0, 0);
    }

    l = xrow_sum(l);
    if (c < 2) {
      LAS float* mg = (LAS float*)(L + L_MRG) + (wave * 2 + c) * 256 + 4 * g;
#pragma unroll
      for (int t = 0; t < 16; ++t) *(LAS f32x4*)(mg + 16 * t) = acc[t];
      if (g == 0) { LAS float* ml = (LAS float*)(L + L_ML) + (wave * 2 + c) * 2; ml[0] = m; ml[1] = l; }
    }
    wg_barrier();
    if (tid < 144) {
      const int head = tid < 128 ? tid >> 6 : (tid - 128) >> 3;
      const LAS float* ml = (const LAS float*)(L + L_ML) + head * 2;
      const float m0 = ml[0], m1 = ml[4], m2 = ml[8], m3 = ml[12];
      const float M = fmaxf(fmaxf(m0, m1), fmaxf(m2, m3));
      const float e0 = __builtin_amdgcn_exp2f(m0 - M), e1 = __builtin_amdgcn_exp2f(m1 - M), e2 = __builtin_amdgcn_exp2f(m2 - M), e3 = __builtin_amdgcn_exp2f(m3 - M);
      u32x4 o; int dst;
      if (tid < 128) {
        const int ch = tid & 63;
        const LAS f32x4* mg = (const LAS f32x4*)(L + L_MRG) + head * 64 + ch;
        const f32x4 v = (mg[0] * e0 + mg[128] * e1) + (mg[256] * e2 + mg[384] * e3);
        o = u32x4{nosent(v[0]), nosent(v[1]), nosent(v[2]), nosent(v[3])};
        dst = (grp * 16 + head * 8 + (ch >> 3)) * INBOXB + (j * 9 + (ch & 7)) * 16;
      } else {
        const float Ls = (ml[1] * e0 + ml[5] * e1) + (ml[9] * e2 + ml[13] * e3);
        o = u32x4{nosent(M), nosent(Ls), 0u, 0u};
        dst = (grp * 16 + (tid - 128)) * INBOXB + (j * 9 + 8) * 16;
      }
      __builtin_amdgcn_raw_buffer_store_b128(o, rs_in, dst, 0, 16);
    }
    if (!wait_lds_ge(ctl + C_FULL + ((NT + (wave >> 1)) & (NSLOT - 1)), NT + (wave >> 1) + 1)) return;
    asm volatile("" ::: "memory");
    unsigned wp[4][8];
    {
      const LAS unsigned char* S = L + L_RING + ((NT + (wave >> 1)) & (NSLOT - 1)) * TILEB + (wave & 1) * 8 * 896 + (lane < 56 ? lane : 0) * 16;
#pragma unroll
      for (int r = 0; r < 4; ++r) {
        const u32x4 a = *(const LAS u32x4*)(S + (2 * r) * 896), bb = *(const LAS u32x4*)(S + (2 * r + 1) * 896);
#pragma unroll
        for (int e = 0; e < 4; ++e) { wp[r][2 * e] = __builtin_amdgcn_perm(bb[e], a[e], 0x05040100u); wp[r][2 * e + 1] = __builtin_amdgcn_perm(bb[e], a[e], 0x07060302u); }
      }
    }
    {
      const int p = lane >> 2, q = lane & 3;
      const int src = wg * INBOXB + (p * 9 + (q < 2 ? 2 * wave + q : 8)) * 16;
      u32x4 v;
      const u64 t0 = rt();
      for (;;) {
        asm volatile("" ::: "memory");
        v = __builtin_amdgcn_raw_buffer_load_b128(rs_in, src, 0, 16);
        const bool ok = v[0] != SENT && v[1] != SENT && v[2] != SENT && v[3] != SENT;
        if (__all(ok)) break;
        if ((long long)(rt() - t0) > TCAP) return;
      }
      const float mp = dpp<QB2>(__uint_as_float(v[0])), lp = dpp<QB2>(__uint_as_float(v[1]));
      float M = mp; M = fmaxf(M, dpp<ROR4>(M)); M = fmaxf(M, dpp<ROR8>(M)); M = xrow_max(M);
      const float e = __builtin_amdgcn_exp2f(mp - M);
      float Ls = e * lp; Ls += dpp<ROR4>(Ls); Ls += dpp<ROR8>(Ls); Ls = xrow_sum(Ls);
      const float inv = 1.f / Ls;
      float o[4];
#pragma unroll
      for (int k = 0; k < 4; ++k) { float t = e * __uint_as_float(v[k]); t += dpp<ROR4>(t); t += dpp<ROR8>(t); o[k] = xrow_sum(t) * inv; }
      const unsigned pk0 = pack2(o[0], o[1]), pk1 = pack2(o[2], o[3]);
      unsigned op[4];
      op[0] = __builtin_amdgcn_mov_dpp(pk0, QB0, 0xf, 0xf, true); op[1] = __builtin_amdgcn_mov_dpp(pk1, QB0, 0xf, 0xf, true);
      op[2] = __builtin_amdgcn_mov_dpp(pk0, QB1, 0xf, 0xf, true); op[3] = __builtin_amdgcn_mov_dpp(pk1, QB1, 0xf, 0xf, true);
      float y[8];
#pragma unroll
      for (int k = 0; k < 8; ++k) { float t = 0.f;
#pragma unroll
        for (int r = 0; r < 4; ++r) t = dot2(wp[r][k], op[r], t);
        y[k] = t; }
      if (lane < 56) {
        LAS float* yp = (LAS float*)(L + L_YP) + wave * DM + lane * 8;
        *(LAS f32x4*)yp = f32x4{y[0], y[1], y[2], y[3]}; *(LAS f32x4*)(yp + 4) = f32x4{y[4], y[5], y[6], y[7]};
      }
    }
    wg_barrier();
    if (tid < 112) {
      const LAS f32x4* yp = (const LAS f32x4*)(L + L_YP) + tid;
      const f32x4 a = (yp[0] + yp[112]) + (yp[224] + yp[336]);
      const f32x4 bp = *((const LAS f32x4*)(L + L_BP) + tid);
      const float gg = *(const LAS float*)(L + L_CTL + C_G * 4);
      const f32x4 y = a * gg + bp;
      const u32x4 o = {nosent(y[0]), nosent(y[1]), nosent(y[2]), nosent(y[3])};
      __builtin_amdgcn_raw_buffer_store_b128(o, rs_in, (b * 128 + tid) * INBOXB + (144 + (wg & 127)) * 16, 0, 16);
    }
    if (wave == 2 && (wg & 127) < 112) {
      const int src = wg * INBOXB + (144 + lane) * 16;
      u32x4 v0, v1;
      const u64 t0 = rt();
      for (;;) {
        asm volatile("" ::: "memory");
        v0 = __builtin_amdgcn_raw_buffer_load_b128(rs_in, src, 0, 16);
        v1 = __builtin_amdgcn_raw_buffer_load_b128(rs_in, src + 64 * 16, 0, 16);
        const bool ok = v0[0] != SENT && v0[1] != SENT && v0[2] != SENT && v0[3] != SENT && v1[0] != SENT && v1[1] != SENT && v1[2] != SENT && v1[3] != SENT;
        if (__all(ok)) break;
        if ((long long)(rt() - t0) > TCAP) return;
      }
      float s[4];
#pragma unroll
      for (int k = 0; k < 4; ++k) {
        float t = __uint_as_float(v0[k]) + __uint_as_float(v1[k]);
        t += dpp<QX1>(t); t += dpp<ROR2>(t); t += dpp<ROR4>(t); t += dpp<ROR8>(t);
        s[k] = xrow_sum(t);
      }
      if (lane == 0) {
        const u32x2 o = {pack2(s[0], s[1]), pack2(s[2], s[3])};
        *(GAS u32x2*)(out + b * DM + (wg & 127) * 4) = o;
        if ((wg & 127) == 0) __hip_atomic_store(epoch, ep + 1, __ATOMIC_RELAXED, __HIP_MEMORY_SCOPE_AGENT);
      }
    }
  } else if (wave < NCONS + NLOAD) {
    const int lw = wave - NCONS;
    unsigned voff[8];
    {
      const unsigned v0 = (lane >> 5) * 512 + (((lane & 31) ^ ((lane >> 5) << 3)) * 16);
#pragma unroll
      for (int i = 0; i < 8; ++i) voff[i] = v0 + i * 1024;
    }
    const char* kb = Kc + ((size_t)grp * SK + (size_t)j * (SK / NSPLIT)) * (DH * 2);
    const char* vb = Vc + ((size_t)grp * SK + (size_t)j * (SK / NSPLIT)) * (DH * 2);
    const unsigned ring = (unsigned)(size_t)(L + L_RING);
    wg_barrier();
    int pub = lw;
    for (int n = lw; n < NT + NLOAD; n += NLOAD) {
      const int slot = n & (NSLOT - 1);
      if (n >= NSLOT && (unsigned)__builtin_amdgcn_readfirstlane(ctl[C_FREE + slot]) < (unsigned)(n - NSLOT + 1)) {
        asm volatile("s_waitcnt vmcnt(0)" ::: "memory");
        for (; pub < n; pub += NLOAD) ctl[C_FULL + (pub & (NSLOT - 1))] = pub + 1;
        if (!wait_lds_ge(ctl + C_FREE + slot, n - NSLOT + 1)) return;
      }
      asm volatile("" ::: "memory");
      if (n < NT) {
        issue_tile(kb + (size_t)n * (TK * DH * 2), vb + (size_t)n * (TK * DH * 2), ring + slot * TILEB, voff);
        if (pub < n) { asm volatile("s_waitcnt vmcnt(16)" ::: "memory"); ctl[C_FULL + (pub & (NSLOT - 1))] = pub + 1; pub += NLOAD; }
      } else {
        issue_wo(wo + ((size_t)(kvh * 512 + j * 32 + lw * 16)) * (DM * 2), ring + slot * TILEB, lane * 16);
        if (pub < n) { asm volatile("s_waitcnt vmcnt(14)" ::: "memory"); ctl[C_FULL + (pub & (NSLOT - 1))] = pub + 1; pub += NLOAD; }
      }
    }
    wg_barrier();
    asm volatile("s_waitcnt vmcnt(0)" ::: "memory");
    ctl[C_FULL + (pub & (NSLOT - 1))] = pub + 1;
    wg_barrier();
  } else {
    const u64 ep = __hip_atomic_load(epoch, __ATOMIC_RELAXED, __HIP_MEMORY_SCOPE_AGENT);
    wg_barrier();
    float gsum = 0.f;
    if (lane < 56) {
      const u32x4 xv = *(const GAS u32x4*)(xb + lane * 8), gv = *(const GAS u32x4*)(gw + lane * 8);
#pragma unroll
      for (int e = 0; e < 4; ++e) gsum += rbf(bflo(xv[e]) * bflo(gv[e])) + rbf(bfhi(xv[e]) * bfhi(gv[e]));
    }
    gsum += dpp<QX1>(gsum); gsum += dpp<ROR2>(gsum); gsum += dpp<ROR4>(gsum); gsum += dpp<ROR8>(gsum); gsum = xrow_sum(gsum);
    const float gate = rbf(1.f / (1.f + __expf(-rbf(gsum)))), omg = rbf(1.f - gate);
    const int idx = kvh * 2 + (j >> 3);
    float y[8];
#pragma unroll
    for (int k = 0; k < 8; ++k) y[k] = 0.f;
    if (idx < 14) {
      const int ub = (j & 7) * 14 + idx, rg = lane >> 1, cc = lane & 1;
      u32x4 wv[14]; unsigned xv[14];
#pragma unroll
      for (int i = 0; i < 14; ++i) { const int row = 32 * i + rg; wv[i] = ldnt(w1 + (size_t)row * FF + ub * 16 + cc * 8); xv[i] = xb[row]; }
      u32x4 w2v[16];
#pragma unroll
      for (int u = 0; u < 16; ++u) w2v[u] = ldnt(w2 + (size_t)(ub * 16 + u) * DM + (lane < 56 ? lane : 0) * 8);
      float h[8];
#pragma unroll
      for (int e = 0; e < 8; ++e) h[e] = 0.f;
#pragma unroll
      for (int i = 0; i < 14; ++i) {
        const float xf = bf2f(xv[i]);
#pragma unroll
        for (int e = 0; e < 4; ++e) { h[2 * e] = fmaf(xf, bflo(wv[i][e]), h[2 * e]); h[2 * e + 1] = fmaf(xf, bfhi(wv[i][e]), h[2 * e + 1]); }
      }
      float hs[16];
#pragma unroll
      for (int e = 0; e < 8; ++e) {
        float t = h[e]; t += dpp<ROR2>(t); t += dpp<ROR4>(t); t += dpp<ROR8>(t); t = rbf(xrow_sum(t));
        t = rbf(t / (1.f + __expf(-t)));
        const float o = dpp<QX1>(t);
        hs[e] = cc ? o : t; hs[8 + e] = cc ? t : o;
      }
#pragma unroll
      for (int u = 0; u < 16; ++u)
#pragma unroll
        for (int e = 0; e < 4; ++e) { y[2 * e] = fmaf(hs[u], bflo(w2v[u][e]), y[2 * e]); y[2 * e + 1] = fmaf(hs[u], bfhi(w2v[u][e]), y[2 * e + 1]); }
    }
    if (lane < 56) {
      LAS float* bp = (LAS float*)(L + L_BP) + lane * 8;
      *(LAS f32x4*)bp = f32x4{y[0] * omg, y[1] * omg, y[2] * omg, y[3] * omg}; *(LAS f32x4*)(bp + 4) = f32x4{y[4] * omg, y[5] * omg, y[6] * omg, y[7] * omg};
    }
    if (lane == 0) *(LAS float*)(L + L_CTL + C_G * 4) = gate;
    {
      const u64 tg = (ep + 1) & 0xFFFFFFFFFFFFull;
      const u64 t0 = rt();
      for (;;) {
        const u64 v0 = __hip_atomic_load(qg + (b * 8 + (lane >> 4)) * 512 + (lane & 15) * 32, __ATOMIC_RELAXED, __HIP_MEMORY_SCOPE_AGENT);
        const u64 v1 = __hip_atomic_load(qg + (b * 8 + 4 + (lane >> 4)) * 512 + (lane & 15) * 32, __ATOMIC_RELAXED, __HIP_MEMORY_SCOPE_AGENT);
        if (__all((v0 >> 16) == tg && (v1 >> 16) == tg)) break;
        if ((long long)(rt() - t0) > TCAP) return;
        __builtin_amdgcn_s_sleep(8);
      }
    }
    wg_barrier();
    wg_barrier();
  }
}

}

extern "C" void kernel_launch(void* const* d_in, const int* in_sizes, int n_in, void* d_out, int out_size, void* d_ws, size_t ws_size,
                              hipStream_t stream) {
  (void)in_sizes; (void)n_in; (void)out_size; (void)ws_size;
  fused_decode<<<NWG, NTHREADS, 0, stream>>>((const u16*)d_in[0], (const char*)d_in[1], (const char*)d_in[2], (const u16*)d_in[3],
                                             (const char*)d_in[4], (const u16*)d_in[6], (const u16*)d_in[7], (const u16*)d_in[8],
                                             (u16*)d_out, (char*)d_ws);
}
```

```cpp
#define KPOL " sc1 nt"
#define VPOL " sc1 nt"
#include <hip/hip_runtime.h>
#include <hip/hip_bf16.h>
#include <stdint.h>

#define GAS __attribute__((address_space(1)))
#define LAS __attribute__((address_space(3)))
typedef unsigned short u16;
typedef unsigned long long u64;
typedef unsigned u32x4 __attribute__((ext_vector_type(4)));
typedef unsigned u32x2 __attribute__((ext_vector_type(2)));
typedef float f32x4 __attribute__((ext_vector_type(4)));
typedef short s16x4 __attribute__((ext_vector_type(4)));
typedef __bf16 bf16x8 __attribute__((ext_vector_type(8)));
typedef __bf16 bf16x2 __attribute__((ext_vector_type(2)));

namespace {

constexpr int DM = 448, HQ = 16, HKV = 8, SK = 32768, DH = 256, FF = 1792;
constexpr int NWG = 256, NSPLIT = 16, TK = 16, NT = SK / NSPLIT / TK;
constexpr int NCONS = 4, NLOAD = 2, NTHREADS = 7 * 64;
constexpr int PSTR = 1056;
constexpr int TILEB = 16 * PSTR;
constexpr int NSLOT = 8;
constexpr int L_RING = 0;
constexpr int L_MRG = NSLOT * TILEB;
constexpr int L_ML = L_MRG + 4 * 2 * 256 * 4;
constexpr int L_YP = L_ML + 64;
constexpr int L_BP = L_YP + 4 * DM * 4;
constexpr int L_Q = L_BP + DM * 4;
constexpr int L_QP = L_Q + 1024;
constexpr int L_CTL = L_QP + 512;
constexpr int LDS_BYTES = L_CTL + 256;
static_assert(LDS_BYTES <= 160 * 1024, "LDS");
enum { C_FULL = 0, C_FREE = 8, C_QCNT = 16, C_QRDY = 17, C_G = 18 };
constexpr size_t W_EPOCH = 0;
constexpr size_t W_QG = 4096;
constexpr size_t W_INBOX = W_QG + 16 * 512 * 8;
constexpr int INBOXB = 272 * 16;
constexpr size_t W_END = W_INBOX + (size_t)NWG * INBOXB;
constexpr unsigned SENT = 0xFFFFFFFFu;
constexpr long long TCAP = 5000000;

__device__ __forceinline__ u64 rt() { return __builtin_amdgcn_s_memrealtime(); }
#define STAMP(i) do { } while (0)
__device__ __forceinline__ float bf2f(unsigned v) { return __uint_as_float(v << 16); }
__device__ __forceinline__ float bflo(unsigned v) { return __uint_as_float(v << 16); }
__device__ __forceinline__ float bfhi(unsigned v) { return __uint_as_float(v & 0xffff0000u); }
__device__ __forceinline__ unsigned f2bf(float f) { return (unsigned)__builtin_bit_cast(u16, (__bf16)f); }
__device__ __forceinline__ float rbf(float f) { return (float)(__bf16)f; }
__device__ __forceinline__ unsigned pack2(float lo, float hi) { bf16x2 r = {(__bf16)lo, (__bf16)hi}; return __builtin_bit_cast(unsigned, r); }
__device__ __forceinline__ float dot2(unsigned a, unsigned b, float c) {
  return __builtin_amdgcn_fdot2_f32_bf16(__builtin_bit_cast(bf16x2, a), __builtin_bit_cast(bf16x2, b), c, false);
}
template <int CTRL> __device__ __forceinline__ float dpp(float x) {
  return __builtin_bit_cast(float, __builtin_amdgcn_mov_dpp(__builtin_bit_cast(int, x), CTRL, 0xf, 0xf, true));
}
constexpr int ROR2 = 0x122, ROR4 = 0x124, ROR8 = 0x128, QX1 = 0xB1, QB0 = 0x00, QB1 = 0x55, QB2 = 0xAA;
__device__ __forceinline__ float xrow_max(float x) {
  auto s = __builtin_amdgcn_permlane16_swap(__float_as_uint(x), __float_as_uint(x), false, false);
  x = fmaxf(__uint_as_float(s[0]), __uint_as_float(s[1]));
  auto t = __builtin_amdgcn_permlane32_swap(__float_as_uint(x), __float_as_uint(x), false, false);
  return fmaxf(__uint_as_float(t[0]), __uint_as_float(t[1]));
}
__device__ __forceinline__ float xrow_sum(float x) {
  auto s = __builtin_amdgcn_permlane16_swap(__float_as_uint(x), __float_as_uint(x), false, false);
  x = __uint_as_float(s[0]) + __uint_as_float(s[1]);
  auto t = __builtin_amdgcn_permlane32_swap(__float_as_uint(x), __float_as_uint(x), false, false);
  return __uint_as_float(t[0]) + __uint_as_float(t[1]);
}
__device__ __forceinline__ unsigned nosent(float f) { unsigned u = __float_as_uint(f); return u == SENT ? 0x7fc00000u : u; }
__device__ __forceinline__ void lds_wait() { asm volatile("s_waitcnt lgkmcnt(0)" ::: "memory"); }
__device__ __forceinline__ void wg_barrier() { asm volatile("s_waitcnt lgkmcnt(0)\n\ts_barrier" ::: "memory"); }
__device__ __forceinline__ u32x4 ldnt(const void* p) { return __builtin_nontemporal_load((const GAS u32x4*)p); }

__device__ __forceinline__ bool wait_lds_ge(volatile LAS unsigned* w, unsigned need) {
  if ((unsigned)__builtin_amdgcn_readfirstlane(*w) >= need) return true;
  const u64 t0 = rt();
  for (;;) {
    __builtin_amdgcn_s_sleep(1);
    if ((unsigned)__builtin_amdgcn_readfirstlane(*w) >= need) return true;
    if ((long long)(rt() - t0) > TCAP) return false;
  }
}

__device__ __forceinline__ void issue_tile(const char* kb, const char* vb, unsigned dst, const unsigned (&voff)[8]) {
  unsigned keep;
  asm volatile(
      "s_mov_b32 %0, m0\n\t"
      "s_mov_b32 m0, %1\n\ts_nop 0\n\tglobal_load_lds_dwordx4 %4, %2" KPOL "\n\t"
      "s_add_u32 m0, m0, 1056\n\ts_nop 0\n\tglobal_load_lds_dwordx4 %5, %2" KPOL "\n\t"
      "s_add_u32 m0, m0, 1056\n\ts_nop 0\n\tglobal_load_lds_dwordx4 %6, %2" KPOL "\n\t"
      "s_add_u32 m0, m0, 1056\n\ts_nop 0\n\tglobal_load_lds_dwordx4 %7, %2" KPOL "\n\t"
      "s_add_u32 m0, m0, 1056\n\ts_nop 0\n\tglobal_load_lds_dwordx4 %8, %2" KPOL "\n\t"
      "s_add_u32 m0, m0, 1056\n\ts_nop 0\n\tglobal_load_lds_dwordx4 %9, %2" KPOL "\n\t"
      "s_add_u32 m0, m0, 1056\n\ts_nop 0\n\tglobal_load_lds_dwordx4 %10, %2" KPOL "\n\t"
      "s_add_u32 m0, m0, 1056\n\ts_nop 0\n\tglobal_load_lds_dwordx4 %11, %2" KPOL "\n\t"
      "s_add_u32 m0, m0, 1056\n\ts_nop 0\n\tglobal_load_lds_dwordx4 %4, %3" VPOL "\n\t"
      "s_add_u32 m0, m0, 1056\n\ts_nop 0\n\tglobal_load_lds_dwordx4 %5, %3" VPOL "\n\t"
      "s_add_u32 m0, m0, 1056\n\ts_nop 0\n\tglobal_load_lds_dwordx4 %6, %3" VPOL "\n\t"
      "s_add_u32 m0, m0, 1056\n\ts_nop 0\n\tglobal_load_lds_dwordx4 %7, %3" VPOL "\n\t"
      "s_add_u32 m0, m0, 1056\n\ts_nop 0\n\tglobal_load_lds_dwordx4 %8, %3" VPOL "\n\t"
      "s_add_u32 m0, m0, 1056\n\ts_nop 0\n\tglobal_load_lds_dwordx4 %9, %3" VPOL "\n\t"
      "s_add_u32 m0, m0, 1056\n\ts_nop 0\n\tglobal_load_lds_dwordx4 %10, %3" VPOL "\n\t"
      "s_add_u32 m0, m0, 1056\n\ts_nop 0\n\tglobal_load_lds_dwordx4 %11, %3" VPOL "\n\t"
      "s_mov_b32 m0, %0"
      : "=&s"(keep)
      : "s"(dst), "s"(kb), "s"(vb), "v"(voff[0]), "v"(voff[1]), "v"(voff[2]), "v"(voff[3]), "v"(voff[4]), "v"(voff[5]), "v"(voff[6]), "v"(voff[7])
      : "memory", "scc");
}
__device__ __forceinline__ void issue_wo(const char* src, unsigned dst, unsigned voff) {
#pragma unroll
  for (int i = 0; i < 14; ++i) {
    unsigned keep;
    asm volatile("s_mov_b32 %0, m0\n\ts_mov_b32 m0, %1\n\ts_nop 0\n\tglobal_load_lds_dwordx4 %3, %2 nt\n\ts_mov_b32 m0, %0"
                 : "=&s"(keep) : "s"(dst + i * 1024), "s"(src + i * 1024), "v"(voff) : "memory");
  }
}

__global__ void __launch_bounds__(NTHREADS, 2)
fused_decode(const u16* __restrict__ x, const char* __restrict__ Kc, const char* __restrict__ Vc, const u16* __restrict__ wq,
             const char* __restrict__ wo, const u16* __restrict__ w1, const u16* __restrict__ w2, const u16* __restrict__ gw,
             u16* __restrict__ out, char* __restrict__ ws) {
  __shared__ __attribute__((aligned(16))) unsigned char lds_raw[LDS_BYTES];
  LAS unsigned char* L = (LAS unsigned char*)lds_raw;
  volatile LAS unsigned* ctl = (volatile LAS unsigned*)(L + L_CTL);
  const int tid = threadIdx.x, lane = tid & 63, wave = __builtin_amdgcn_readfirstlane(tid >> 6);
  const int wg = blockIdx.x, grp = wg >> 4, j = wg & 15, b = grp >> 3, kvh = grp & 7;
  const u16* xb = x + b * DM;
  GAS u64* epoch = (GAS u64*)(ws + W_EPOCH + b * 256);
  GAS u64* qg = (GAS u64*)(ws + W_QG);
  const __amdgpu_buffer_rsrc_t rs_in = __builtin_amdgcn_make_buffer_rsrc(ws + W_INBOX, 0, NWG * INBOXB, 0x00020000);

  if (wave < NCONS) {
    if (wave == 0) STAMP(0);
    const u64 ep = __hip_atomic_load(epoch, __ATOMIC_RELAXED, __HIP_MEMORY_SCOPE_AGENT);
    {
      const u32x4 s4 = {SENT, SENT, SENT, SENT};
      __builtin_amdgcn_raw_buffer_store_b128(s4, rs_in, wg * INBOXB + tid * 16, 0, 16);
      if (tid < 16) __builtin_amdgcn_raw_buffer_store_b128(s4, rs_in, wg * INBOXB + (256 + tid) * 16, 0, 16);
    }
    const int cb = 2 * (j & 7) + (j >> 3), rg = lane >> 2, cc = lane & 3;
    u32x4 wv[7]; unsigned xv[7];
#pragma unroll
    for (int i = 0; i < 7; ++i) {
      const int row = 112 * wave + 16 * i + rg;
      wv[i] = ldnt(wq + (size_t)row * (HQ * DH) + kvh * 512 + cb * 32 + cc * 8);
      xv[i] = xb[row];
    }
    if (tid < 32) ctl[tid] = 0;
    wg_barrier();
    {
      float a[8];
#pragma unroll
      for (int e = 0; e < 8; ++e) a[e] = 0.f;
#pragma unroll
      for (int i = 0; i < 7; ++i) {
        const float xf = bf2f(xv[i]);
#pragma unroll
        for (int e = 0; e < 4; ++e) { a[2 * e] = fmaf(xf, bflo(wv[i][e]), a[2 * e]); a[2 * e + 1] = fmaf(xf, bfhi(wv[i][e]), a[2 * e + 1]); }
      }
#pragma unroll
      for (int e = 0; e < 8; ++e) { float t = a[e]; t += dpp<ROR4>(t); t += dpp<ROR8>(t); a[e] = xrow_sum(t); }
      LAS float* qp = (LAS float*)(L + L_QP) + wave * 32;
      if (lane < 4) { *(LAS f32x4*)(qp + lane * 8) = f32x4{a[0], a[1], a[2], a[3]}; *(LAS f32x4*)(qp + lane * 8 + 4) = f32x4{a[4], a[5], a[6], a[7]}; }
      asm volatile("s_waitcnt vmcnt(0) lgkmcnt(0)" ::: "memory");
      if (wave == 0) STAMP(1);
      unsigned old = 0;
      if (lane == 0) old = __hip_atomic_fetch_add((LAS unsigned*)(L + L_CTL) + C_QCNT, 1u, __ATOMIC_RELAXED, __HIP_MEMORY_SCOPE_WORKGROUP);
      old = __builtin_amdgcn_readfirstlane(old);
      const u64 tag = ((ep + 1) & 0xFFFFFFFFFFFFull) << 16;
      if (old == NCONS - 1) {
        asm volatile("" ::: "memory");
        if (lane < 32) {
          const LAS float* q0 = (const LAS float*)(L + L_QP);
          const float s = (q0[lane] + q0[32 + lane]) + (q0[64 + lane] + q0[96 + lane]);
          __hip_atomic_store(qg + grp * 512 + cb * 32 + lane, tag | f2bf(s), __ATOMIC_RELAXED, __HIP_MEMORY_SCOPE_AGENT);
        }
      }
      if (wave == 0) {
        const u64 t0 = rt();
        for (;;) {
          bool ok = true; u64 v[8];
#pragma unroll
          for (int i = 0; i < 8; ++i) { v[i] = __hip_atomic_load(qg + grp * 512 + i * 64 + lane, __ATOMIC_RELAXED, __HIP_MEMORY_SCOPE_AGENT); ok &= (v[i] >> 16) == (tag >> 16); }
          if (__all(ok)) {
#pragma unroll
            for (int i = 0; i < 8; ++i) *(LAS u16*)(L + L_Q + (i * 64 + lane) * 2) = (u16)v[i];
            break;
          }
          if ((long long)(rt() - t0) > TCAP) return;
          __builtin_amdgcn_s_sleep(1);
        }
        lds_wait();
        ctl[C_QRDY] = 1;
        STAMP(2);
      }
    }
    if (!wait_lds_ge(ctl + C_QRDY, 1)) return;
    asm volatile("" ::: "memory");
    const int g = lane >> 4, c = lane & 15;
    bf16x8 qf[8];
#pragma unroll
    for (int s = 0; s < 8; ++s) qf[s] = *(const LAS bf16x8*)(L + L_Q + ((c & 1) * 256 + 32 * s + 8 * g) * 2);

    const int odd = c & 1;
    const unsigned kA = (c >> 1) * PSTR + odd * 512 + g * 16 + odd * 128, kB = (c >> 1) * PSTR + odd * 512 + g * 16 + (odd ^ 1) * 128;
    const int vkey = 4 * g + (c >> 2), vodd = vkey & 1;
    const unsigned vbase = 8 * PSTR + (vkey >> 1) * PSTR + vodd * 512 + (c & 3) * 8;
    const unsigned vA = vbase + vodd * 128, vB = vbase + (vodd ^ 1) * 128;
    constexpr float C2 = 0.0625f * 1.4426950408889634f;

    f32x4 acc[16];
#pragma unroll
    for (int t = 0; t < 16; ++t) acc[t] = f32x4{0.f, 0.f, 0.f, 0.f};
    float m = -INFINITY, l = 0.f;

    for (int n = wave; n < NT; n += NCONS) {
      const int slot = n & (NSLOT - 1);
      if (!wait_lds_ge(ctl + C_FULL + slot, n + 1)) return;
      asm volatile("" ::: "memory");
      if (n == 0) STAMP(3);
      if (n == 8) STAMP(19);
      if (n == 16) STAMP(20);
      if (n == 64) STAMP(21);
      LAS unsigned char* S = L + L_RING + slot * TILEB;
      bf16x8 kf[8]; s16x4 vf[16];
#pragma unroll
      for (int s = 0; s < 8; ++s) kf[s] = *(const LAS bf16x8*)(S + ((s & 2) ? kB : kA) + 64 * (s & 1) + 256 * (s >> 2));
#pragma unroll
      for (int t = 0; t < 16; ++t) vf[t] = __builtin_amdgcn_ds_read_tr16_b64_v4i16((LAS s16x4*)(S + ((t & 4) ? vB : vA) + 32 * (t & 3) + 256 * (t >> 3)));
      f32x4 sc = {0.f, 0.f, 0.f, 0.f};
#pragma unroll
      for (int s = 0; s < 8; ++s) sc = __builtin_amdgcn_mfma_f32_16x16x32_bf16(kf[s], qf[s], sc, 0, 0, 0);
      lds_wait();
      ctl[C_FREE + slot] = n + 1;
      const float tmax = fmaxf(fmaxf(sc[0], sc[1]), fmaxf(sc[2], sc[3])) * C2;
      if (__any(tmax > m)) {
        const float mn = fmaxf(m, xrow_max(tmax));
        const float al = __builtin_amdgcn_exp2f(m - mn);
        l *= al;
#pragma unroll
        for (int t = 0; t < 16; ++t) acc[t] *= al;
        m = mn;
      }
      const float p0 = __builtin_amdgcn_exp2f(fmaf(sc[0], C2, -m)), p1 = __builtin_amdgcn_exp2f(fmaf(sc[1], C2, -m));
      const float p2 = __builtin_amdgcn_exp2f(fmaf(sc[2], C2, -m)), p3 = __builtin_amdgcn_exp2f(fmaf(sc[3], C2, -m));
      l += (p0 + p1) + (p2 + p3);
      const u32x2 pp = {pack2(p0, p1), pack2(p2, p3)};
      const s16x4 pf = __builtin_bit_cast(s16x4, pp);
#pragma unroll
      for (int t = 0; t < 16; ++t) acc[t] = __builtin_amdgcn_mfma_f32_16x16x16bf16_1k(vf[t], pf, acc[t], 0, 0, 0);
    }

    STAMP(4 + wave);
    l = xrow_sum(l);
    if (c < 2) {
      LAS float* mg = (LAS float*)(L + L_MRG) + (wave * 2 + c) * 256 + 4 * g;
#pragma unroll
      for (int t = 0; t < 16; ++t) *(LAS f32x4*)(mg + 16 * t) = acc[t];
      if (g == 0) { LAS float* ml = (LAS float*)(L + L_ML) + (wave * 2 + c) * 2; ml[0] = m; ml[1] = l; }
    }
    wg_barrier();
    if (wave == 0) STAMP(8);
    if (tid < 144) {
      const int head = tid < 128 ? tid >> 6 : (tid - 128) >> 3;
      const LAS float* ml = (const LAS float*)(L + L_ML) + head * 2;
      const float m0 = ml[0], m1 = ml[4], m2 = ml[8], m3 = ml[12];
      const float M = fmaxf(fmaxf(m0, m1), fmaxf(m2, m3));
      const float e0 = __builtin_amdgcn_exp2f(m0 - M), e1 = __builtin_amdgcn_exp2f(m1 - M), e2 = __builtin_amdgcn_exp2f(m2 - M), e3 = __builtin_amdgcn_exp2f(m3 - M);
      u32x4 o; int dst;
      if (tid < 128) {
        const int ch = tid & 63;
        const LAS f32x4* mg = (const LAS f32x4*)(L + L_MRG) + head * 64 + ch;
        const f32x4 v = (mg[0] * e0 + mg[128] * e1) + (mg[256] * e2 + mg[384] * e3);
        o = u32x4{nosent(v[0]), nosent(v[1]), nosent(v[2]), nosent(v[3])};
        dst = (grp * 16 + head * 8 + (ch >> 3)) * INBOXB + (j * 9 + (ch & 7)) * 16;
      } else {
        const float Ls = (ml[1] * e0 + ml[5] * e1) + (ml[9] * e2 + ml[13] * e3);
        o = u32x4{nosent(M), nosent(Ls), 0u, 0u};
        dst = (grp * 16 + (tid - 128)) * INBOXB + (j * 9 + 8) * 16;
      }
      __builtin_amdgcn_raw_buffer_store_b128(o, rs_in, dst, 0, 16);
    }
    if (!wait_lds_ge(ctl + C_FULL + ((NT + (wave >> 1)) & (NSLOT - 1)), NT + (wave >> 1) + 1)) return;
    asm volatile("" ::: "memory");
    if (wave == 0) STAMP(9);
    unsigned wp[4][8];
    {
      const LAS unsigned char* S = L + L_RING + ((NT + (wave >> 1)) & (NSLOT - 1)) * TILEB + (wave & 1) * 8 * 896 + (lane < 56 ? lane : 0) * 16;
#pragma unroll
      for (int r = 0; r < 4; ++r) {
        const u32x4 a = *(const LAS u32x4*)(S + (2 * r) * 896), bb = *(const LAS u32x4*)(S + (2 * r + 1) * 896);
#pragma unroll
        for (int e = 0; e < 4; ++e) { wp[r][2 * e] = __builtin_amdgcn_perm(bb[e], a[e], 0x05040100u); wp[r][2 * e + 1] = __builtin_amdgcn_perm(bb[e], a[e], 0x07060302u); }
      }
    }
    {
      const int p = lane >> 2, q = lane & 3;
      const int src = wg * INBOXB + (p * 9 + (q < 2 ? 2 * wave + q : 8)) * 16;
      u32x4 v;
      const u64 t0 = rt();
      for (;;) {
        asm volatile("" ::: "memory");
        v = __builtin_amdgcn_raw_buffer_load_b128(rs_in, src, 0, 16);
        const bool ok = v[0] != SENT && v[1] != SENT && v[2] != SENT && v[3] != SENT;
        if (__all(ok)) break;
        if ((long long)(rt() - t0) > TCAP) return;
      }
      if (wave == 0) STAMP(10);
      const float mp = dpp<QB2>(__uint_as_float(v[0])), lp = dpp<QB2>(__uint_as_float(v[1]));
      float M = mp; M = fmaxf(M, dpp<ROR4>(M)); M = fmaxf(M, dpp<ROR8>(M)); M = xrow_max(M);
      const float e = __builtin_amdgcn_exp2f(mp - M);
      float Ls = e * lp; Ls += dpp<ROR4>(Ls); Ls += dpp<ROR8>(Ls); Ls = xrow_sum(Ls);
      const float inv = 1.f / Ls;
      float o[4];
#pragma unroll
      for (int k = 0; k < 4; ++k) { float t = e * __uint_as_float(v[k]); t += dpp<ROR4>(t); t += dpp<ROR8>(t); o[k] = xrow_sum(t) * inv; }
      const unsigned pk0 = pack2(o[0], o[1]), pk1 = pack2(o[2], o[3]);
      unsigned op[4];
      op[0] = __builtin_amdgcn_mov_dpp(pk0, QB0, 0xf, 0xf, true); op[1] = __builtin_amdgcn_mov_dpp(pk1, QB0, 0xf, 0xf, true);
      op[2] = __builtin_amdgcn_mov_dpp(pk0, QB1, 0xf, 0xf, true); op[3] = __builtin_amdgcn_mov_dpp(pk1, QB1, 0xf, 0xf, true);
      float y[8];
#pragma unroll
      for (int k = 0; k < 8; ++k) { float t = 0.f;
#pragma unroll
        for (int r = 0; r < 4; ++r) t = dot2(wp[r][k], op[r], t);
        y[k] = t; }
      if (lane < 56) {
        LAS float* yp = (LAS float*)(L + L_YP) + wave * DM + lane * 8;
        *(LAS f32x4*)yp = f32x4{y[0], y[1], y[2], y[3]}; *(LAS f32x4*)(yp + 4) = f32x4{y[4], y[5], y[6], y[7]};
      }
    }
    wg_barrier();
    if (wave == 0) STAMP(11);
    if (tid < 112) {
      const LAS f32x4* yp = (const LAS f32x4*)(L + L_YP) + tid;
      const f32x4 a = (yp[0] + yp[112]) + (yp[224] + yp[336]);
      const f32x4 bp = *((const LAS f32x4*)(L + L_BP) + tid);
      const float gg = *(const LAS float*)(L + L_CTL + C_G * 4);
      const f32x4 y = a * gg + bp;
      const u32x4 o = {nosent(y[0]), nosent(y[1]), nosent(y[2]), nosent(y[3])};
      __builtin_amdgcn_raw_buffer_store_b128(o, rs_in, (b * 128 + tid) * INBOXB + (144 + (wg & 127)) * 16, 0, 16);
    }
    if (wave == 2 && (wg & 127) < 112) {
      const int src = wg * INBOXB + (144 + lane) * 16;
      u32x4 v0, v1;
      const u64 t0 = rt();
      for (;;) {
        asm volatile("" ::: "memory");
        v0 = __builtin_amdgcn_raw_buffer_load_b128(rs_in, src, 0, 16);
        v1 = __builtin_amdgcn_raw_buffer_load_b128(rs_in, src + 64 * 16, 0, 16);
        const bool ok = v0[0] != SENT && v0[1] != SENT && v0[2] != SENT && v0[3] != SENT && v1[0] != SENT && v1[1] != SENT && v1[2] != SENT && v1[3] != SENT;
        if (__all(ok)) break;
        if ((long long)(rt() - t0) > TCAP) return;
      }
      STAMP(12);
      float s[4];
#pragma unroll
      for (int k = 0; k < 4; ++k) {
        float t = __uint_as_float(v0[k]) + __uint_as_float(v1[k]);
        t += dpp<QX1>(t); t += dpp<ROR2>(t); t += dpp<ROR4>(t); t += dpp<ROR8>(t);
        s[k] = xrow_sum(t);
      }
      if (lane == 0) {
        const u32x2 o = {pack2(s[0], s[1]), pack2(s[2], s[3])};
        *(GAS u32x2*)(out + b * DM + (wg & 127) * 4) = o;
        if ((wg & 127) == 0) __hip_atomic_store(epoch, ep + 1, __ATOMIC_RELAXED, __HIP_MEMORY_SCOPE_AGENT);
      }
      STAMP(13);
    }
  } else if (wave < NCONS + NLOAD) {
    const int lw = wave - NCONS;
    unsigned voff[8];
    {
      const unsigned v0 = (lane >> 5) * 512 + (((lane & 31) ^ ((lane >> 5) << 3)) * 16);
#pragma unroll
      for (int i = 0; i < 8; ++i) voff[i] = v0 + i * 1024;
    }
    const char* kb = Kc + ((size_t)grp * SK + (size_t)j * (SK / NSPLIT)) * (DH * 2);
    const char* vb = Vc + ((size_t)grp * SK + (size_t)j * (SK / NSPLIT)) * (DH * 2);
    const unsigned ring = (unsigned)(size_t)(L + L_RING);
    wg_barrier();
    int pub = lw;
    for (int n = lw; n < NT + NLOAD; n += NLOAD) {
      const int slot = n & (NSLOT - 1);
      if (n >= NSLOT && (unsigned)__builtin_amdgcn_readfirstlane(ctl[C_FREE + slot]) < (unsigned)(n - NSLOT + 1)) {
        asm volatile("s_waitcnt vmcnt(0)" ::: "memory");
        for (; pub < n; pub += NLOAD) ctl[C_FULL + (pub & (NSLOT - 1))] = pub + 1;
        if (!wait_lds_ge(ctl + C_FREE + slot, n - NSLOT + 1)) return;
      }
      asm volatile("" ::: "memory");
      if (n < NT) {
        issue_tile(kb + (size_t)n * (TK * DH * 2), vb + (size_t)n * (TK * DH * 2), ring + slot * TILEB, voff);
        if (pub < n) { asm volatile("s_waitcnt vmcnt(16)" ::: "memory"); ctl[C_FULL + (pub & (NSLOT - 1))] = pub + 1; pub += NLOAD; }
      } else {
        issue_wo(wo + ((size_t)(kvh * 512 + j * 32 + lw * 16)) * (DM * 2), ring + slot * TILEB, lane * 16);
        if (pub < n) { asm volatile("s_waitcnt vmcnt(14)" ::: "memory"); ctl[C_FULL + (pub & (NSLOT - 1))] = pub + 1; pub += NLOAD; }
      }
    }
    wg_barrier();
    asm volatile("s_waitcnt vmcnt(0)" ::: "memory");
    ctl[C_FULL + (pub & (NSLOT - 1))] = pub + 1;
    wg_barrier();
  } else {
    const u64 ep = __hip_atomic_load(epoch, __ATOMIC_RELAXED, __HIP_MEMORY_SCOPE_AGENT);
    wg_barrier();
    float gsum = 0.f;
    if (lane < 56) {
      const u32x4 xv = *(const GAS u32x4*)(xb + lane * 8), gv = *(const GAS u32x4*)(gw + lane * 8);
#pragma unroll
      for (int e = 0; e < 4; ++e) gsum += rbf(bflo(xv[e]) * bflo(gv[e])) + rbf(bfhi(xv[e]) * bfhi(gv[e]));
    }
    gsum += dpp<QX1>(gsum); gsum += dpp<ROR2>(gsum); gsum += dpp<ROR4>(gsum); gsum += dpp<ROR8>(gsum); gsum = xrow_sum(gsum);
    const float gate = rbf(1.f / (1.f + __expf(-rbf(gsum)))), omg = rbf(1.f - gate);
    const int idx = kvh * 2 + (j >> 3);
    float y[8];
#pragma unroll
    for (int k = 0; k < 8; ++k) y[k] = 0.f;
    if (idx < 14) {
      const int ub = (j & 7) * 14 + idx, rg = lane >> 1, cc = lane & 1;
      u32x4 wv[14]; unsigned xv[14];
#pragma unroll
      for (int i = 0; i < 14; ++i) { const int row = 32 * i + rg; wv[i] = ldnt(w1 + (size_t)row * FF + ub * 16 + cc * 8); xv[i] = xb[row]; }
      u32x4 w2v[16];
#pragma unroll
      for (int u = 0; u < 16; ++u) w2v[u] = ldnt(w2 + (size_t)(ub * 16 + u) * DM + (lane < 56 ? lane : 0) * 8);
      float h[8];
#pragma unroll
      for (int e = 0; e < 8; ++e) h[e] = 0.f;
#pragma unroll
      for (int i = 0; i < 14; ++i) {
        const float xf = bf2f(xv[i]);
#pragma unroll
        for (int e = 0; e < 4; ++e) { h[2 * e] = fmaf(xf, bflo(wv[i][e]), h[2 * e]); h[2 * e + 1] = fmaf(xf, bfhi(wv[i][e]), h[2 * e + 1]); }
      }
      float hs[16];
#pragma unroll
      for (int e = 0; e < 8; ++e) {
        float t = h[e]; t += dpp<ROR2>(t); t += dpp<ROR4>(t); t += dpp<ROR8>(t); t = rbf(xrow_sum(t));
        t = rbf(t / (1.f + __expf(-t)));
        const float o = dpp<QX1>(t);
        hs[e] = cc ? o : t; hs[8 + e] = cc ? t : o;
      }
#pragma unroll
      for (int u = 0; u < 16; ++u)
#pragma unroll
        for (int e = 0; e < 4; ++e) { y[2 * e] = fmaf(hs[u], bflo(w2v[u][e]), y[2 * e]); y[2 * e + 1] = fmaf(hs[u], bfhi(w2v[u][e]), y[2 * e + 1]); }
    }
    if (lane < 56) {
      LAS float* bp = (LAS float*)(L + L_BP) + lane * 8;
      *(LAS f32x4*)bp = f32x4{y[0] * omg, y[1] * omg, y[2] * omg, y[3] * omg}; *(LAS f32x4*)(bp + 4) = f32x4{y[4] * omg, y[5] * omg, y[6] * omg, y[7] * omg};
    }
    if (lane == 0) *(LAS float*)(L + L_CTL + C_G * 4) = gate;
    STAMP(17);
    {
      const u64 tg = (ep + 1) & 0xFFFFFFFFFFFFull;
      const u64 t0 = rt();
      for (;;) {
        const u64 v0 = __hip_atomic_load(qg + (b * 8 + (lane >> 4)) * 512 + (lane & 15) * 32, __ATOMIC_RELAXED, __HIP_MEMORY_SCOPE_AGENT);
        const u64 v1 = __hip_atomic_load(qg + (b * 8 + 4 + (lane >> 4)) * 512 + (lane & 15) * 32, __ATOMIC_RELAXED, __HIP_MEMORY_SCOPE_AGENT);
        if (__all((v0 >> 16) == tg && (v1 >> 16) == tg)) break;
        if ((long long)(rt() - t0) > TCAP) return;
        __builtin_amdgcn_s_sleep(8);
      }
    }
    STAMP(18);
    wg_barrier();
    wg_barrier();
  }
}

}

extern "C" void kernel_launch(void* const* d_in, const int* in_sizes, int n_in, void* d_out, int out_size, void* d_ws, size_t ws_size,
                              hipStream_t stream) {
  (void)in_sizes; (void)n_in; (void)out_size; (void)ws_size;
  fused_decode<<<NWG, NTHREADS, 0, stream>>>((const u16*)d_in[0], (const char*)d_in[1], (const char*)d_in[2], (const u16*)d_in[3],
                                             (const char*)d_in[4], (const u16*)d_in[6], (const u16*)d_in[7], (const u16*)d_in[8],
                                             (u16*)d_out, (char*)d_ws);
}
```

```cpp
#include <hip/hip_runtime.h>
#include <hip/hip_bf16.h>
#include <stdint.h>

#define GAS __attribute__((address_space(1)))
#define LAS __attribute__((address_space(3)))
typedef unsigned short u16;
typedef unsigned long long u64;
typedef unsigned u32x4 __attribute__((ext_vector_type(4)));
typedef unsigned u32x2 __attribute__((ext_vector_type(2)));
typedef float f32x4 __attribute__((ext_vector_type(4)));
typedef short s16x4 __attribute__((ext_vector_type(4)));
typedef __bf16 bf16x8 __attribute__((ext_vector_type(8)));
typedef __bf16 bf16x2 __attribute__((ext_vector_type(2)));

namespace {

constexpr int DM = 448, HQ = 16, SK = 32768, DH = 256, FF = 1792;
constexpr int NWG = 256;
constexpr int NT = 128;
constexpr int WIN = 8;
constexpr int NCONS = 4, NLOAD = 2, NTHREADS = 7 * 64;
constexpr int PSTR = 1056;
constexpr int TILEB = 16 * PSTR;
constexpr int NSLOT = 8;
constexpr int L_RING = 0;
constexpr int L_SC = NSLOT * TILEB;
constexpr int L_ML = L_SC + 4 * 4096;
constexpr int L_BP = L_ML + 64;
constexpr int L_Q = L_BP + DM * 4;
constexpr int L_QP = L_Q + 1024;
constexpr int L_CTL = L_QP + 512;
constexpr int LDS_BYTES = L_CTL + 256;
static_assert(LDS_BYTES <= 160 * 1024, "LDS");
enum { C_FULL = 0, C_FREE = 8, C_QCNT = 16, C_QRDY = 17, C_G = 18, C_WO = 20   };
constexpr size_t W_EPOCH = 0;
constexpr size_t W_CLR = 1024;
constexpr size_t W_QG = 4096;
constexpr size_t W_INBOX = W_QG + 16 * 512 * 8;
constexpr int IN1 = 16 * 8 + 16, IN2 = 128;
constexpr int INBOXB = (IN1 + IN2) * 16;
constexpr unsigned SENT = 0x7F800001u;
constexpr long long TCAP = 100000000;

__device__ __forceinline__ u64 rt() { return __builtin_amdgcn_s_memrealtime(); }
__device__ __forceinline__ float bflo(unsigned v) { return __uint_as_float(v << 16); }
__device__ __forceinline__ float bfhi(unsigned v) { return __uint_as_float(v & 0xffff0000u); }
__device__ __forceinline__ unsigned f2bf(float f) { return (unsigned)__builtin_bit_cast(u16, (__bf16)f); }
__device__ __forceinline__ float rbf(float f) { return (float)(__bf16)f; }
__device__ __forceinline__ unsigned pack2(float lo, float hi) { bf16x2 r = {(__bf16)lo, (__bf16)hi}; return __builtin_bit_cast(unsigned, r); }
__device__ __forceinline__ float dot2(unsigned a, unsigned b, float c) {
  return __builtin_amdgcn_fdot2_f32_bf16(__builtin_bit_cast(bf16x2, a), __builtin_bit_cast(bf16x2, b), c, false);
}
template <int CTRL> __device__ __forceinline__ float dpp(float x) {
  return __builtin_bit_cast(float, __builtin_amdgcn_mov_dpp(__builtin_bit_cast(int, x), CTRL, 0xf, 0xf, true));
}
constexpr int ROR2 = 0x122, ROR4 = 0x124, ROR8 = 0x128, QX1 = 0xB1, QB2 = 0xAA;
__device__ __forceinline__ float xrow_max(float x) {
  auto s = __builtin_amdgcn_permlane16_swap(__float_as_uint(x), __float_as_uint(x), false, false);
  x = fmaxf(__uint_as_float(s[0]), __uint_as_float(s[1]));
  auto t = __builtin_amdgcn_permlane32_swap(__float_as_uint(x), __float_as_uint(x), false, false);
  return fmaxf(__uint_as_float(t[0]), __uint_as_float(t[1]));
}
__device__ __forceinline__ float xrow_sum(float x) {
  auto s = __builtin_amdgcn_permlane16_swap(__float_as_uint(x), __float_as_uint(x), false, false);
  x = __uint_as_float(s[0]) + __uint_as_float(s[1]);
  auto t = __builtin_amdgcn_permlane32_swap(__float_as_uint(x), __float_as_uint(x), false, false);
  return __uint_as_float(t[0]) + __uint_as_float(t[1]);
}
template <int N> __device__ __forceinline__ void xrow_sum_n(float (&x)[N]) {
#pragma unroll
  for (int k = 0; k < N; ++k) { auto s = __builtin_amdgcn_permlane16_swap(__float_as_uint(x[k]), __float_as_uint(x[k]), false, false); x[k] = __uint_as_float(s[0]) + __uint_as_float(s[1]); }
#pragma unroll
  for (int k = 0; k < N; ++k) { auto s = __builtin_amdgcn_permlane32_swap(__float_as_uint(x[k]), __float_as_uint(x[k]), false, false); x[k] = __uint_as_float(s[0]) + __uint_as_float(s[1]); }
}
template <int CTRL, int N> __device__ __forceinline__ void dpp_add_n(float (&x)[N]) {
#pragma unroll
  for (int k = 0; k < N; ++k) x[k] += dpp<CTRL>(x[k]);
}
__device__ __forceinline__ u32x4 bits4(f32x4 v) { return __builtin_bit_cast(u32x4, v); }
__device__ __forceinline__ bool full4(u32x4 v) { return (v[0] != SENT) & (v[1] != SENT) & (v[2] != SENT) & (v[3] != SENT); }
__device__ __forceinline__ void lds_wait() { asm volatile("s_waitcnt lgkmcnt(0)" ::: "memory"); }
__device__ __forceinline__ void wg_barrier() {
  __builtin_amdgcn_sched_barrier(0);
  asm volatile("s_waitcnt lgkmcnt(0)\n\ts_barrier" ::: "memory");
  __builtin_amdgcn_sched_barrier(0);
}
__device__ __forceinline__ u32x4 ldnt(const void* p) { return __builtin_nontemporal_load((const GAS u32x4*)p); }
__device__ __forceinline__ void poll1(const char* base, unsigned o0, u32x4& a) {
  asm volatile("global_load_dwordx4 %0, %1, %2 sc1\n\ts_waitcnt vmcnt(0)" : "=&v"(a) : "v"(o0), "s"(base) : "memory");
}
__device__ __forceinline__ void poll2(const char* base, unsigned o0, unsigned o1, u32x4& a, u32x4& b) {
  asm volatile("global_load_dwordx4 %0, %2, %4 sc1\n\tglobal_load_dwordx4 %1, %3, %4 sc1\n\ts_waitcnt vmcnt(0)"
               : "=&v"(a), "=&v"(b) : "v"(o0), "v"(o1), "s"(base) : "memory");
}
__device__ __forceinline__ void poll4(const char* base, unsigned o, u32x4 (&v)[4]) {
  asm volatile("global_load_dwordx4 %0, %4, %5 sc1\n\tglobal_load_dwordx4 %1, %4, %5 offset:1024 sc1\n\tglobal_load_dwordx4 %2, %4, %5 offset:2048 sc1\n\t"
               "global_load_dwordx4 %3, %4, %5 offset:3072 sc1\n\ts_waitcnt vmcnt(0)"
               : "=&v"(v[0]), "=&v"(v[1]), "=&v"(v[2]), "=&v"(v[3]) : "v"(o), "s"(base) : "memory");
}
__device__ __forceinline__ unsigned ldsu(volatile LAS unsigned* w) { return (unsigned)__builtin_amdgcn_readfirstlane(*w); }
__device__ __forceinline__ bool expired(unsigned& spins, u64& t0) {
  if ((++spins & 63u) != 0u) return false;
  const u64 t = rt();
  if (spins == 64u) { t0 = t; return false; }
  return (long long)(t - t0) > TCAP;
}
__device__ __forceinline__ bool wait_lds_ge(volatile LAS unsigned* w, unsigned need) {
  unsigned spins = 0; u64 t0 = 0;
  while (ldsu(w) < need) { __builtin_amdgcn_s_sleep(1); if (expired(spins, t0)) return false; }
  return true;
}
#define PIECE(V) "s_add_u32 m0, m0, 1056\n\ts_nop 0\n\tglobal_load_lds_dwordx4 " V ", %2 nt\n\t"
__device__ __forceinline__ void issue_half(const char* src, unsigned dst, const unsigned (&voff)[8]) {
  unsigned keep;
  asm volatile(
      "s_mov_b32 %0, m0\n\t"
      "s_mov_b32 m0, %1\n\ts_nop 0\n\tglobal_load_lds_dwordx4 %3, %2 nt\n\t"
      PIECE("%4") PIECE("%5") PIECE("%6") PIECE("%7") PIECE("%8") PIECE("%9") PIECE("%10")
      "s_mov_b32 m0, %0"
      : "=&s"(keep)
      : "s"(dst), "s"(src), "v"(voff[0]), "v"(voff[1]), "v"(voff[2]), "v"(voff[3]), "v"(voff[4]), "v"(voff[5]), "v"(voff[6]), "v"(voff[7])
      : "memory", "scc");
}
template <int N> __device__ __forceinline__ void vm_wait() { asm volatile("s_waitcnt vmcnt(%0)" :: "i"(N) : "memory"); }
__device__ __forceinline__ void issue_wo(const char* src, unsigned dst, unsigned voff) {
#pragma unroll
  for (int i = 0; i < 14; ++i) {
    unsigned keep;
    asm volatile("s_mov_b32 %0, m0\n\ts_mov_b32 m0, %1\n\ts_nop 0\n\tglobal_load_lds_dwordx4 %3, %2 nt\n\ts_mov_b32 m0, %0"
                 : "=&s"(keep) : "s"(dst + i * 1024), "s"(src + i * 1024), "v"(voff) : "memory");
  }
}

__global__ void __launch_bounds__(NTHREADS, 2)
fused_decode(const u16* __restrict__ x, const char* __restrict__ Kc, const char* __restrict__ Vc, const u16* __restrict__ wq,
             const char* __restrict__ wo, const u16* __restrict__ w1, const u16* __restrict__ w2, const u16* __restrict__ gw,
             u16* out, char* ws) {
  __shared__ __attribute__((aligned(16))) unsigned char lds_raw[LDS_BYTES];
  LAS unsigned char* L = (LAS unsigned char*)lds_raw;
  volatile LAS unsigned* ctl = (volatile LAS unsigned*)(L + L_CTL);
  const int tid = threadIdx.x, lane = tid & 63, wave = __builtin_amdgcn_readfirstlane(tid >> 6);
  const int wg = blockIdx.x, grp = wg >> 4, j = wg & 15, b = grp >> 3, kvh = grp & 7;

  if (wave < NCONS) {
    const u16* xb = x + b * DM;
    for (int k = 0; k < 6; ++k) __builtin_amdgcn_s_sleep(1);
    const int cb = 2 * (j & 7) + (j >> 3), cc = lane & 3;
    const int row0 = (wave * 14 + (lane < 56 ? lane >> 2 : 0)) * 8;
    u32x4 wv[8];
#pragma unroll
    for (int i = 0; i < 8; ++i) wv[i] = ldnt(wq + (size_t)(row0 + i) * (HQ * DH) + kvh * 512 + cb * 32 + cc * 8);
    u32x4 xv = *(const GAS u32x4*)(xb + row0);
    if (tid < 32) ctl[tid] = 0u;
    wg_barrier();
#pragma unroll
    for (int i = 0; i < 8; ++i) asm volatile("" : "+v"(wv[i]));
    asm volatile("" : "+v"(xv));
    GAS u64* qg = (GAS u64*)(ws + W_QG);
    const __amdgpu_buffer_rsrc_t rs_in = __builtin_amdgcn_make_buffer_rsrc(ws + W_INBOX, 0, NWG * INBOXB, 0x00020000);
    GAS u64* epoch = (GAS u64*)(ws + W_EPOCH + b * 256);
    const u64 ep = *(const __attribute__((address_space(4))) u64*)(ws + W_EPOCH + b * 256);
    const u64 tag48 = (ep + 1) & 0xFFFFFFFFFFFFull;
    unsigned qold;
    {
      float a[8];
#pragma unroll
      for (int e = 0; e < 8; ++e) a[e] = 0.f;
#pragma unroll
      for (int i = 0; i < 8; ++i) {
        const float xf = lane < 56 ? ((i & 1) ? bfhi(xv[i >> 1]) : bflo(xv[i >> 1])) : 0.f;
#pragma unroll
        for (int e = 0; e < 4; ++e) { a[2 * e] = fmaf(xf, bflo(wv[i][e]), a[2 * e]); a[2 * e + 1] = fmaf(xf, bfhi(wv[i][e]), a[2 * e + 1]); }
      }
#pragma unroll
      for (int e = 0; e < 8; ++e) { float t = a[e]; t += dpp<ROR4>(t); t += dpp<ROR8>(t); a[e] = xrow_sum(t); }
      LAS float* qp = (LAS float*)(L + L_QP) + wave * 32;
      if (lane < 4) { *(LAS f32x4*)(qp + lane * 8) = f32x4{a[0], a[1], a[2], a[3]}; *(LAS f32x4*)(qp + lane * 8 + 4) = f32x4{a[4], a[5], a[6], a[7]}; }
      lds_wait();
      unsigned old = 0;
      if (lane == 0) old = __hip_atomic_fetch_add((LAS unsigned*)(L + L_CTL) + C_QCNT, 1u, __ATOMIC_RELAXED, __HIP_MEMORY_SCOPE_WORKGROUP);
      old = __builtin_amdgcn_readfirstlane(old);
      const u64 tag = tag48 << 16;
      if (old == NCONS - 1) {
        asm volatile("" ::: "memory");
        if (lane < 32) {
          const LAS float* q0 = (const LAS float*)(L + L_QP);
          const float s = (q0[lane] + q0[32 + lane]) + (q0[64 + lane] + q0[96 + lane]);
          __hip_atomic_store(qg + grp * 512 + cb * 32 + lane, tag | f2bf(s), __ATOMIC_RELAXED, __HIP_MEMORY_SCOPE_AGENT);
        }
      }
      qold = old;
    }
    const int g = lane >> 4, c = lane & 15;
    const int odd = c & 1;
    const unsigned kA = (c >> 1) * PSTR + odd * 512 + g * 16 + odd * 128, kB = (c >> 1) * PSTR + odd * 512 + g * 16 + (odd ^ 1) * 128;
    const int vkey = 4 * g + (c >> 2), vodd = vkey & 1;
    const unsigned vbase = (vkey >> 1) * PSTR + vodd * 512 + (c & 3) * 8;
    const unsigned vA = vbase + vodd * 128, vB = vbase + (vodd ^ 1) * 128;
    constexpr float C2 = 0.0625f * 1.4426950408889634f;
    u32x4 fr[16];
    auto fetch_k = [&](int n) -> bool {
      const int slot = n & (NSLOT - 1);
      if (!wait_lds_ge(ctl + C_FULL + slot, n + 1)) return false;
      asm volatile("" ::: "memory");
      LAS unsigned char* S = L + L_RING + slot * TILEB;
#pragma unroll
      for (int h = 0; h < 2; ++h)
#pragma unroll
        for (int s = 0; s < 8; ++s) fr[h * 8 + s] = *(const LAS u32x4*)(S + h * 8 * PSTR + ((s & 2) ? kB : kA) + 64 * (s & 1) + 256 * (s >> 2));
      lds_wait();
      ctl[C_FREE + slot] = n + 1;
      return true;
    };
    auto fetch_v = [&](int n) -> bool {
      const int slot = n & (NSLOT - 1);
      if (!wait_lds_ge(ctl + C_FULL + slot, n + 1)) return false;
      asm volatile("" ::: "memory");
      LAS unsigned char* S = L + L_RING + slot * TILEB;
#pragma unroll
      for (int t = 0; t < 16; ++t) {
        const unsigned o = ((t & 4) ? vB : vA) + 32 * (t & 3) + 256 * (t >> 3);
        const u32x2 lo = __builtin_bit_cast(u32x2, __builtin_amdgcn_ds_read_tr16_b64_v4i16((LAS s16x4*)(S + o)));
        const u32x2 hi = __builtin_bit_cast(u32x2, __builtin_amdgcn_ds_read_tr16_b64_v4i16((LAS s16x4*)(S + 8 * PSTR + o)));
        fr[t] = u32x4{lo[0], lo[1], hi[0], hi[1]};
      }
      lds_wait();
      ctl[C_FREE + slot] = n + 1;
      return true;
    };
    if (!fetch_k(wave)) return;
    {
      if (qold == NCONS - 1) {
        for (int k = 0; k < 8; ++k) __builtin_amdgcn_s_sleep(2);
        const unsigned tlo = (unsigned)tag48 & 0xffffu, thi = (unsigned)(tag48 >> 16);
        unsigned spins = 0; u64 t0 = 0;
        for (;;) {
          bool ok = true; u32x4 v[4];
          poll4(ws + W_QG + grp * 4096, lane * 16, v);
#pragma unroll
          for (int i = 0; i < 4; ++i) ok &= ((v[i][0] >> 16) == tlo) & (v[i][1] == thi) & ((v[i][2] >> 16) == tlo) & (v[i][3] == thi);
          if (__all(ok)) {
#pragma unroll
            for (int i = 0; i < 4; ++i) *(LAS unsigned*)(L + L_Q + (i * 128 + lane * 2) * 2) = (v[i][0] & 0xffffu) | (v[i][2] << 16);
            lds_wait();
            ctl[C_QRDY] = 1;
            break;
          }
          if (expired(spins, t0)) return;
        }
      } else if (!wait_lds_ge(ctl + C_QRDY, 1)) return;
    }
    asm volatile("" ::: "memory");
    bf16x8 qf[8];
#pragma unroll
    for (int s = 0; s < 8; ++s) qf[s] = *(const LAS bf16x8*)(L + L_Q + ((c & 1) * 256 + 32 * s + 8 * g) * 2);

    LAS unsigned char* sc_l = L + L_SC + wave * 4096 + (g * 2 + (c & 1)) * 16;
    float m;
    {
      float mx = -INFINITY;
      for (int n = wave, t = 0;; ++t) {
        f32x4 s0 = {0.f, 0.f, 0.f, 0.f}, s1 = {0.f, 0.f, 0.f, 0.f};
#pragma unroll
        for (int s = 0; s < 8; ++s) {
          s0 = __builtin_amdgcn_mfma_f32_16x16x32_bf16(__builtin_bit_cast(bf16x8, fr[s]), qf[s], s0, 0, 0, 0);
          s1 = __builtin_amdgcn_mfma_f32_16x16x32_bf16(__builtin_bit_cast(bf16x8, fr[8 + s]), qf[s], s1, 0, 0, 0);
        }
        mx = fmaxf(mx, fmaxf(fmaxf(fmaxf(s0[0], s0[1]), fmaxf(s0[2], s0[3])), fmaxf(fmaxf(s1[0], s1[1]), fmaxf(s1[2], s1[3]))));
        if (c < 2) { *(LAS f32x4*)(sc_l + t * 256) = s0; *(LAS f32x4*)(sc_l + t * 256 + 128) = s1; }
        n += NCONS;
        if (n >= NT / 2) break;
        if (!fetch_k(n)) return;
      }
      m = xrow_max(mx) * C2;
    }
    f32x4 accA = {0.f, 0.f, 0.f, 0.f}, accB = {0.f, 0.f, 0.f, 0.f};
    float l = 0.f;
    const int u = c >> 1;
    lds_wait();
    for (int n = NT / 2 + wave, t = 0; n < NT; n += NCONS, ++t) {
      const f32x4 s0 = *(const LAS f32x4*)(sc_l + t * 256), s1 = *(const LAS f32x4*)(sc_l + t * 256 + 128);
      float p[8];
#pragma unroll
      for (int k = 0; k < 4; ++k) { p[k] = __builtin_amdgcn_exp2f(fmaf(s0[k], C2, -m)); p[4 + k] = __builtin_amdgcn_exp2f(fmaf(s1[k], C2, -m)); }
      l += ((p[0] + p[1]) + (p[2] + p[3])) + ((p[4] + p[5]) + (p[6] + p[7]));
      u32x4 pp = {pack2(p[0], p[1]), pack2(p[2], p[3]), pack2(p[4], p[5]), pack2(p[6], p[7])};
      asm volatile("" : "+v"(pp), "+v"(l));
      if (!fetch_v(n)) return;
#pragma unroll
      for (int tt = 0; tt < 8; ++tt) {
        const u32x4 pm = {u == tt ? pp[0] : 0u, u == tt ? pp[1] : 0u, u == tt ? pp[2] : 0u, u == tt ? pp[3] : 0u};
        accA = __builtin_amdgcn_mfma_f32_16x16x32_bf16(__builtin_bit_cast(bf16x8, fr[tt]), __builtin_bit_cast(bf16x8, pm), accA, 0, 0, 0);
        accB = __builtin_amdgcn_mfma_f32_16x16x32_bf16(__builtin_bit_cast(bf16x8, fr[tt + 8]), __builtin_bit_cast(bf16x8, pm), accB, 0, 0, 0);
      }
    }
    l = xrow_sum(l);
    {
      LAS float* mg = (LAS float*)(L + L_SC + wave * 4096) + (c & 1) * 256 + 16 * u + 4 * g;
      *(LAS f32x4*)mg = accA; *(LAS f32x4*)(mg + 128) = accB;
      if (lane < 2) { LAS float* ml = (LAS float*)(L + L_ML) + (wave * 2 + c) * 2; ml[0] = m; ml[1] = l; }
    }
    wg_barrier();
    if (tid < 144) {
      const int head = tid < 128 ? tid >> 6 : (tid - 128) >> 3;
      const LAS float* ml = (const LAS float*)(L + L_ML) + head * 2;
      const float m0 = ml[0], m1 = ml[4], m2 = ml[8], m3 = ml[12];
      const float M = fmaxf(fmaxf(m0, m1), fmaxf(m2, m3));
      const float e0 = __builtin_amdgcn_exp2f(m0 - M), e1 = __builtin_amdgcn_exp2f(m1 - M), e2 = __builtin_amdgcn_exp2f(m2 - M), e3 = __builtin_amdgcn_exp2f(m3 - M);
      u32x4 o; int dst;
      if (tid < 128) {
        const int ch = tid & 63;
        const LAS f32x4* mg = (const LAS f32x4*)(L + L_SC) + head * 64 + ch;
        o = bits4((mg[0] * e0 + mg[256] * e1) + (mg[512] * e2 + mg[768] * e3));
        dst = (grp * 16 + head * 8 + (ch >> 3)) * INBOXB + (j * 8 + (ch & 7)) * 16;
      } else {
        const float Ls = (ml[1] * e0 + ml[5] * e1) + (ml[9] * e2 + ml[13] * e3);
        o = u32x4{__float_as_uint(M), __float_as_uint(Ls), 0u, 0u};
        dst = (grp * 16 + (tid - 128)) * INBOXB + (128 + j) * 16;
      }
      __builtin_amdgcn_raw_buffer_store_b128(o, rs_in, dst, 0, 16);
    }
    if (!wait_lds_ge(ctl + C_WO + (wave >> 1), 1)) return;
    asm volatile("" ::: "memory");
    unsigned wp[4][8];
    {
      const int wslot = (NT + (wave >> 1)) & (NSLOT - 1);
      const LAS unsigned char* S = L + L_RING + wslot * TILEB + (wave & 1) * 8 * 896 + (lane < 56 ? lane : 0) * 16;
#pragma unroll
      for (int r = 0; r < 4; ++r) {
        const u32x4 a = *(const LAS u32x4*)(S + (2 * r) * 896), bb = *(const LAS u32x4*)(S + (2 * r + 1) * 896);
#pragma unroll
        for (int e = 0; e < 4; ++e) { wp[r][2 * e] = __builtin_amdgcn_perm(bb[e], a[e], 0x05040100u); wp[r][2 * e + 1] = __builtin_amdgcn_perm(bb[e], a[e], 0x07060302u); }
      }
#pragma unroll
      for (int r = 0; r < 4; ++r)
#pragma unroll
        for (int k = 0; k < 8; ++k) asm volatile("" : "+v"(wp[r][k]));
    }
    {
      const int p = lane >> 2, q = lane & 3;
      const unsigned src = wg * INBOXB + (q < 2 ? p * 8 + 2 * wave + q : 128 + p) * 16;
      u32x4 v;
      {
        unsigned spins = 0; u64 t0 = 0;
        bool have = false;
        for (;;) {
          if (!have) { poll1(ws + W_INBOX, src, v); have = full4(v); }
          if (__all(have)) break;
          if (expired(spins, t0)) return;
          __builtin_amdgcn_s_sleep(1);
        }
      }
      const float mp = dpp<QB2>(__uint_as_float(v[0])), lp = dpp<QB2>(__uint_as_float(v[1]));
      float M = fmaxf(mp, dpp<ROR4>(mp)); M = fmaxf(M, dpp<ROR8>(M)); M = xrow_max(M);
      const float e = __builtin_amdgcn_exp2f(mp - M);
      float o[5] = {e * __uint_as_float(v[0]), e * __uint_as_float(v[1]), e * __uint_as_float(v[2]), e * __uint_as_float(v[3]), e * lp};
      dpp_add_n<ROR4>(o); dpp_add_n<ROR8>(o); xrow_sum_n(o);
      const float inv = __builtin_amdgcn_rcpf(o[4]);
#pragma unroll
      for (int k = 0; k < 4; ++k) o[k] *= inv;
      const unsigned pk0 = pack2(o[0], o[1]), pk1 = pack2(o[2], o[3]);
      unsigned op[4];
      op[0] = __builtin_amdgcn_readlane(pk0, 0); op[1] = __builtin_amdgcn_readlane(pk1, 0);
      op[2] = __builtin_amdgcn_readlane(pk0, 1); op[3] = __builtin_amdgcn_readlane(pk1, 1);
      float y[8];
#pragma unroll
      for (int k = 0; k < 8; ++k) { float t = 0.f;
#pragma unroll
        for (int r = 0; r < 4; ++r) t = dot2(wp[r][k], op[r], t);
        y[k] = t; }
      if (lane < 56) {
        LAS float* yp = (LAS float*)(L + L_SC + wave * 4096 + 2048) + lane * 8;
        *(LAS f32x4*)yp = f32x4{y[0], y[1], y[2], y[3]}; *(LAS f32x4*)(yp + 4) = f32x4{y[4], y[5], y[6], y[7]};
      }
    }
    wg_barrier();
    if (tid < 112) {
      const LAS f32x4* yp = (const LAS f32x4*)(L + L_SC + 2048) + tid;
      const f32x4 a = (yp[0] + yp[256]) + (yp[512] + yp[768]);
      const f32x4 bp = *((const LAS f32x4*)(L + L_BP) + tid);
      const float gg = *(const LAS float*)(L + L_CTL + C_G * 4);
      const f32x4 y = a * gg + bp;
      __builtin_amdgcn_raw_buffer_store_b128(bits4(y), rs_in, (b * 128 + tid) * INBOXB + (IN1 + (wg & 127)) * 16, 0, 16);
    }
    if (wave == 2 && (wg & 127) < 112) {
      const int src = wg * INBOXB + (IN1 + lane) * 16;
      u32x4 v0, v1;
      {
        unsigned spins = 0; u64 t0 = 0;
        bool have = false;
        for (;;) {
          if (!have) { poll2(ws + W_INBOX, src, src + 64 * 16, v0, v1); have = full4(v0) & full4(v1); }
          if (__all(have)) break;
          if (expired(spins, t0)) return;
        }
      }
      float s[4];
#pragma unroll
      for (int k = 0; k < 4; ++k) s[k] = __uint_as_float(v0[k]) + __uint_as_float(v1[k]);
      dpp_add_n<QX1>(s); dpp_add_n<ROR2>(s); dpp_add_n<ROR4>(s); dpp_add_n<ROR8>(s); xrow_sum_n(s);
      if (lane == 0) {
        const u64 o = (u64)pack2(s[0], s[1]) | ((u64)pack2(s[2], s[3]) << 32);
        __hip_atomic_store((GAS u64*)(out + b * DM + (wg & 127) * 4), o, __ATOMIC_RELAXED, __HIP_MEMORY_SCOPE_AGENT);
        if ((wg & 127) == 0) __hip_atomic_store(epoch, ep + 1, __ATOMIC_RELAXED, __HIP_MEMORY_SCOPE_AGENT);
      }
    }
  } else if (wave < NCONS + NLOAD) {
    const int lw = wave - NCONS;
    unsigned voff[8];
    {
      const unsigned v0 = (lane >> 5) * 512 + (((lane & 31) ^ ((lane >> 5) << 3)) * 16);
#pragma unroll
      for (int i = 0; i < 8; ++i) voff[i] = v0 + i * 1024;
    }
    const char* kb = Kc + (size_t)grp * SK * (DH * 2);
    const char* vb = Vc + (size_t)grp * SK * (DH * 2);
    const unsigned ring = (unsigned)(size_t)(L + L_RING);
    const size_t koff = ((size_t)j * 2048 + (size_t)lw * 1024) * (DH * 2);
    const char* kp = kb + koff;
    const char* vp = vb + koff;
    unsigned tch0, tch1;
    asm volatile("s_load_dword %0, %2, 0x0\n\ts_load_dword %1, %3, 0x0" : "=&s"(tch0), "=&s"(tch1) : "s"(vp), "s"(wo + ((size_t)(kvh * 512 + j * 32)) * (DM * 2)));
    issue_half(kp, ring + lw * TILEB, voff);
    issue_half(kp + 8192, ring + lw * TILEB + 8 * PSTR, voff);
    asm volatile("s_waitcnt lgkmcnt(0)" :: "s"(tch0), "s"(tch1) : "memory");
    wg_barrier();
    int n = lw + NLOAD;
    for (; n < NT + NLOAD; n += NLOAD) {
      const int slot = n & (NSLOT - 1);
      bool pending = true;
      if (n >= NSLOT && ldsu(ctl + C_FREE + slot) < (unsigned)(n - NSLOT + 1)) {
        vm_wait<0>();
        ctl[C_FULL + ((n - NLOAD) & (NSLOT - 1))] = n - NLOAD + 1; pending = false;
        if (!wait_lds_ge(ctl + C_FREE + slot, n - NSLOT + 1)) return;
      }
      asm volatile("" ::: "memory");
      if (n < NT) {
        const int i = n >> 1;
        const char* src = (i < 32 ? kp : vp) + (size_t)(i & 31) * 16384;
        issue_half(src, ring + slot * TILEB, voff);
        vm_wait<WIN>();
        if (pending) ctl[C_FULL + ((n - NLOAD) & (NSLOT - 1))] = n - NLOAD + 1;
        issue_half(src + 8192, ring + slot * TILEB + 8 * PSTR, voff);
        vm_wait<WIN>();
      } else {
        issue_wo(wo + ((size_t)(kvh * 512 + j * 32 + lw * 16)) * (DM * 2), ring + slot * TILEB, lane * 16);
        vm_wait<14>();
        if (pending) ctl[C_FULL + ((n - NLOAD) & (NSLOT - 1))] = n - NLOAD + 1;
      }
    }
    wg_barrier();
    asm volatile("s_waitcnt vmcnt(0)" ::: "memory");
    ctl[C_WO + lw] = 1;
    wg_barrier();
  } else {
    wg_barrier();
    const u16* xb = x + b * DM;
    GAS u64* clr = (GAS u64*)(ws + W_CLR);
    const __amdgpu_buffer_rsrc_t rs_in = __builtin_amdgcn_make_buffer_rsrc(ws + W_INBOX, 0, NWG * INBOXB, 0x00020000);
    const u64 ep = *(const __attribute__((address_space(4))) u64*)(ws + W_EPOCH + b * 256);
    const u64 tag48 = (ep + 1) & 0xFFFFFFFFFFFFull;
    {
      const u32x4 s4 = {SENT, SENT, SENT, SENT};
#pragma unroll
      for (int i = 0; i < 4; ++i) __builtin_amdgcn_raw_buffer_store_b128(s4, rs_in, wg * INBOXB + (i * 64 + lane) * 16, 0, 16);
      if (lane < 16) __builtin_amdgcn_raw_buffer_store_b128(s4, rs_in, wg * INBOXB + (256 + lane) * 16, 0, 16);
      vm_wait<0>();
      if (lane == 0) __hip_atomic_store(clr + wg, tag48, __ATOMIC_RELAXED, __HIP_MEMORY_SCOPE_AGENT);
    }
    if (!wait_lds_ge(ctl + C_QRDY, 1)) return;
    asm volatile("" ::: "memory");
    {
      unsigned spins = 0; u64 t0 = 0;
      for (;;) {
        const u64 v0 = __hip_atomic_load(clr + b * 128 + lane, __ATOMIC_RELAXED, __HIP_MEMORY_SCOPE_AGENT);
        const u64 v1 = __hip_atomic_load(clr + b * 128 + 64 + lane, __ATOMIC_RELAXED, __HIP_MEMORY_SCOPE_AGENT);
        if (__all(v0 == tag48 && v1 == tag48)) break;
        if (expired(spins, t0)) return;
        __builtin_amdgcn_s_sleep(8);
      }
    }
    float gsum = 0.f;
    if (lane < 56) {
      const u32x4 xv = *(const GAS u32x4*)(xb + lane * 8), gv = *(const GAS u32x4*)(gw + lane * 8);
#pragma unroll
      for (int e = 0; e < 4; ++e) gsum += rbf(bflo(xv[e]) * bflo(gv[e])) + rbf(bfhi(xv[e]) * bfhi(gv[e]));
    }
    gsum += dpp<QX1>(gsum); gsum += dpp<ROR2>(gsum); gsum += dpp<ROR4>(gsum); gsum += dpp<ROR8>(gsum); gsum = xrow_sum(gsum);
    const float gate = rbf(1.f / (1.f + __expf(-rbf(gsum)))), omg = rbf(1.f - gate);
    const int idx = kvh * 2 + (j >> 3);
    float y[8];
#pragma unroll
    for (int k = 0; k < 8; ++k) y[k] = 0.f;
    if (idx < 14) {
      const int ub = (j & 7) * 14 + idx, cc = lane & 1, row0 = (lane < 56 ? lane >> 1 : 0) * 16;
      const u32x4 xa = *(const GAS u32x4*)(xb + row0), xc = *(const GAS u32x4*)(xb + row0 + 8);
      float xf[16];
#pragma unroll
      for (int i = 0; i < 4; ++i) { xf[2 * i] = bflo(xa[i]); xf[2 * i + 1] = bfhi(xa[i]); xf[8 + 2 * i] = bflo(xc[i]); xf[8 + 2 * i + 1] = bfhi(xc[i]); }
      if (lane >= 56) {
#pragma unroll
        for (int i = 0; i < 16; ++i) xf[i] = 0.f;
      }
      u32x4 wv[16], w2v[16];
#pragma unroll
      for (int i = 0; i < 16; ++i) wv[i] = ldnt(w1 + (size_t)(row0 + i) * FF + ub * 16 + cc * 8);
#pragma unroll
      for (int u = 0; u < 16; ++u) w2v[u] = ldnt(w2 + (size_t)(ub * 16 + u) * DM + (lane < 56 ? lane : 0) * 8);
      float h[8];
#pragma unroll
      for (int e = 0; e < 8; ++e) h[e] = 0.f;
#pragma unroll
      for (int i = 0; i < 16; ++i)
#pragma unroll
        for (int e = 0; e < 4; ++e) { h[2 * e] = fmaf(xf[i], bflo(wv[i][e]), h[2 * e]); h[2 * e + 1] = fmaf(xf[i], bfhi(wv[i][e]), h[2 * e + 1]); }
      unsigned hp[8];
      {
        float hs[8];
#pragma unroll
        for (int e = 0; e < 8; ++e) {
          float t = h[e]; t += dpp<ROR2>(t); t += dpp<ROR4>(t); t += dpp<ROR8>(t); t = rbf(xrow_sum(t));
          hs[e] = t / (1.f + __expf(-t));
        }
#pragma unroll
        for (int e = 0; e < 4; ++e) {
          const unsigned pk = pack2(hs[2 * e], hs[2 * e + 1]);
          hp[e] = __builtin_amdgcn_readlane(pk, 0); hp[4 + e] = __builtin_amdgcn_readlane(pk, 1);
        }
      }
#pragma unroll
      for (int r = 0; r < 8; ++r)
#pragma unroll
        for (int e = 0; e < 4; ++e) {
          y[2 * e] = dot2(__builtin_amdgcn_perm(w2v[2 * r + 1][e], w2v[2 * r][e], 0x05040100u), hp[r], y[2 * e]);
          y[2 * e + 1] = dot2(__builtin_amdgcn_perm(w2v[2 * r + 1][e], w2v[2 * r][e], 0x07060302u), hp[r], y[2 * e + 1]);
        }
    }
    if (lane < 56) {
      LAS float* bp = (LAS float*)(L + L_BP) + lane * 8;
      *(LAS f32x4*)bp = f32x4{y[0] * omg, y[1] * omg, y[2] * omg, y[3] * omg}; *(LAS f32x4*)(bp + 4) = f32x4{y[4] * omg, y[5] * omg, y[6] * omg, y[7] * omg};
    }
    if (lane == 0) *(LAS float*)(L + L_CTL + C_G * 4) = gate;
    { const unsigned t0 = *(const GAS unsigned*)(wo + ((size_t)(kvh * 512 + j * 32)) * (DM * 2)); asm volatile("" :: "v"(t0)); }
    wg_barrier();
    wg_barrier();
  }
}

}

extern "C" void kernel_launch(void* const* d_in, const int* in_sizes, int n_in, void* d_out, int out_size, void* d_ws, size_t ws_size,
                              hipStream_t stream) {
  (void)in_sizes; (void)n_in; (void)out_size; (void)ws_size;
  fused_decode<<<NWG, NTHREADS, 0, stream>>>((const u16*)d_in[0], (const char*)d_in[1], (const char*)d_in[2], (const u16*)d_in[3],
                                             (const char*)d_in[4], (const u16*)d_in[6], (const u16*)d_in[7], (const u16*)d_in[8],
                                             (u16*)d_out, (char*)d_ws);
}
```
